```python
import torch

def _softmax(x, axis=-1):
    return torch.softmax(x, dim=axis)
def _gelu(x):
    return torch.nn.functional.gelu(x, approximate="tanh")
def _silu(x):
    return torch.nn.functional.silu(x)
def _sigmoid(x):
    return torch.sigmoid(x)
def _layernorm(x, axis=-1, eps=1e-5):
    m = x.mean(dim=axis, keepdim=True)
    v = x.var(dim=axis, keepdim=True, unbiased=False)
    return (x - m) / torch.sqrt(v + eps)
def _scatter_add(tgt, idx, src, axis=0):
    return tgt.clone().index_add_(axis, idx, src)
def _segment_sum(src, seg, n):
    out = torch.zeros((n,) + src.shape[1:], dtype=src.dtype)
    return out.index_add_(0, seg, src)
def _pad(x, pw):
    flat = tuple(v for pair in reversed(pw) for v in pair)
    return torch.nn.functional.pad(x, flat)

kernel_name = 'interleave_layers__linear_attn_block__swiglu_ffn__prompt'

def setup_inputs():

    _w = (lambda a: (lambda t: t.to(torch.bfloat16) if t.is_floating_point() else t)(torch.as_tensor(a)))
    def _A_setup():
        B, S, D = 1, 2048, 1024
        return {
            "x": _w((lambda *s: torch.randn(*s, dtype=torch.bfloat16) * 0.05)(B, S, D)),
            "wqkv": _w((lambda *s: torch.randn(*s, dtype=torch.bfloat16) * 0.05)(D, 3 * D)),
            "wo": _w((lambda *s: torch.randn(*s, dtype=torch.bfloat16) * 0.05)(D, D)),
        }
    _A_in = _A_setup()

    def _B_setup():
        B, S, D, F = 1, 2048, 1024, 3584
        return {
            "x": _w((lambda *s: torch.randn(*s, dtype=torch.bfloat16) * 0.05)(B, S, D)),
            "w_gate": _w((lambda *s: torch.randn(*s, dtype=torch.bfloat16) * 0.05)(D, F)),
            "w_up": _w((lambda *s: torch.randn(*s, dtype=torch.bfloat16) * 0.05)(D, F)),
            "w_down": _w((lambda *s: torch.randn(*s, dtype=torch.bfloat16) * 0.05)(F, D)),
        }
    _B_in = _B_setup()

    return {
        "A_x": _A_in["x"],
        "A_wqkv": _A_in["wqkv"],
        "A_wo": _A_in["wo"],
        "B_x": _B_in["x"],
        "B_w_gate": _B_in["w_gate"],
        "B_w_up": _B_in["w_up"],
        "B_w_down": _B_in["w_down"],
    }

def reference(A_x, A_wqkv, A_wo, B_x, B_w_gate, B_w_up, B_w_down):
    def _A_ref(x, wqkv, wo):
        B, S, D = x.shape
        H = 32; Dh = D // H
        qkv = (lambda x, *d: x.permute(*d))((x @ wqkv).reshape(B, S, 3, H, Dh), 2, 0, 3, 1, 4)
        q, k, v = qkv[0], qkv[1], qkv[2]
        phi = lambda t: torch.where(t > 0, t + 1, torch.exp(t)) + 1e-6
        pq, pk = phi(q), phi(k)
        kv = torch.einsum("bhsd,bhse->bhde", pk, v)
        z = torch.einsum("bhsd->bhd", pk)
        num = torch.einsum("bhqd,bhde->bhqe", pq, kv)
        den = torch.einsum("bhqd,bhd->bhq", pq, z)[..., None] + 1e-6
        o = num / den
        return (lambda x, *d: x.permute(*d))(o, 0, 2, 1, 3).reshape(B, S, D) @ wo

    def _B_ref(x, w_gate, w_up, w_down):
        g = _silu(x @ w_gate)
        u = x @ w_up
        return (g * u) @ w_down

    x = A_x
    x = x + _A_ref(_layernorm(x, axis=-1), A_wqkv, A_wo)
    x = x + _B_ref(_layernorm(x, axis=-1), B_w_gate, B_w_up, B_w_down)
    x = x + _A_ref(_layernorm(x, axis=-1), A_wqkv, A_wo)
    x = x + _B_ref(_layernorm(x, axis=-1), B_w_gate, B_w_up, B_w_down)
    return x
```

```cpp
#include <hip/hip_runtime.h>
#include <hip/hip_bf16.h>
#include <stdint.h>

namespace {

typedef unsigned short bf16_t;
typedef short bf16x8 __attribute__((ext_vector_type(8)));
typedef short bf16x4 __attribute__((ext_vector_type(4)));
typedef __bf16 bf16x2n __attribute__((ext_vector_type(2)));
typedef float f32x4 __attribute__((ext_vector_type(4)));
typedef float f32x2 __attribute__((ext_vector_type(2)));
typedef unsigned u32x4 __attribute__((ext_vector_type(4)));
typedef unsigned u32x2 __attribute__((ext_vector_type(2)));
#define LAS __attribute__((address_space(3)))
#define DI __device__ __forceinline__

constexpr int SEQ = 2048, DM = 1024, FF = 3584, NH = 32;
constexpr float LN_EPS = 1e-5f;

constexpr size_t MB = 1u << 20;
constexpr size_t WS_XN0 = 0;
constexpr size_t WS_PQ = 4 * MB;
constexpr size_t WS_O = 8 * MB;
constexpr size_t WS_X1 = 12 * MB;
constexpr size_t WS_X2 = 16 * MB;
constexpr size_t WS_H = 20 * MB;
constexpr size_t WS_KVP = 36 * MB;
constexpr size_t WS_ST1 = 40 * MB;
constexpr size_t WS_ST2 = 41 * MB;
constexpr size_t WS_CS = 42 * MB;
constexpr int CS_KP = 8, CS_N = 3 * DM + 2 * FF;
constexpr int KVP_STRIDE = 1056;

DI unsigned cvt_pk(float lo, float hi) { f32x2 v = {lo, hi}; bf16x2n b = __builtin_convertvector(v, bf16x2n); return __builtin_bit_cast(unsigned, b); }
DI float bf_lo(unsigned u) { return __uint_as_float(u << 16); }
DI float bf_hi(unsigned u) { return __uint_as_float(u & 0xffff0000u); }
DI float fast_exp(float x) { return __builtin_amdgcn_exp2f(x * 1.44269504088896341f); }
DI float phi_f(float t) { return (t > 0.f ? t + 1.f : fast_exp(t)) + 1e-6f; }
DI void swap16(unsigned& a, unsigned& b) { auto r = __builtin_amdgcn_permlane16_swap(a, b, false, false); a = r[0]; b = r[1]; }
DI void swap32(unsigned& a, unsigned& b) { auto r = __builtin_amdgcn_permlane32_swap(a, b, false, false); a = r[0]; b = r[1]; }
DI void swap16f(float& a, float& b) { unsigned x = __float_as_uint(a), y = __float_as_uint(b); swap16(x, y); a = __uint_as_float(x); b = __uint_as_float(y); }
DI void swap32f(float& a, float& b) { unsigned x = __float_as_uint(a), y = __float_as_uint(b); swap32(x, y); a = __uint_as_float(x); b = __uint_as_float(y); }
DI float xrow_sum(float x) {
    float a = x, b = x; swap16f(a, b); x = a + b;
    a = x; b = x; swap32f(a, b); return a + b;
}
DI void dma16(unsigned m0v, unsigned voff, __amdgpu_buffer_rsrc_t rs, unsigned soff) {
    asm volatile("s_mov_b32 m0, %0\n\ts_nop 0\n\tbuffer_load_dwordx4 %1, %2, %3 offen lds" ::"s"(m0v), "v"(voff), "s"(rs), "s"(soff) : "memory");
}
template <int N> DI void wait_vm() { asm volatile("s_waitcnt vmcnt(%0)" ::"n"(N) : "memory"); }
DI void wait_lgkm0() { __builtin_amdgcn_s_waitcnt(0xC07F); }
DI void bar() { asm volatile("" ::: "memory"); __builtin_amdgcn_s_barrier(); asm volatile("" ::: "memory"); }
#define SCHED() __builtin_amdgcn_sched_barrier(0)
DI __amdgpu_buffer_rsrc_t mk_rsrc(const void* p, unsigned bytes) { return __builtin_amdgcn_make_buffer_rsrc(const_cast<void*>(p), 0, bytes, 0x00020000); }
DI bf16x8 lds_b128(LAS const char* p) { return *(LAS const bf16x8*)p; }
typedef short v4i16_t __attribute__((ext_vector_type(4)));
DI bf16x4 lds_tr(LAS const char* p) { return __builtin_amdgcn_ds_read_tr16_b64_v4i16((LAS v4i16_t*)p); }
DI bf16x8 cat(bf16x4 a, bf16x4 b) { return __builtin_shufflevector(a, b, 0, 1, 2, 3, 4, 5, 6, 7); }
DI f32x4 mfma(bf16x8 a, bf16x8 b, f32x4 c) { return __builtin_amdgcn_mfma_f32_16x16x32_bf16(a, b, c, 0, 0, 0); }
#define STAMP_DECL do { } while (0)
#define STAMP_ARG
#define STAMP_PASS
#define STAMP(i) do { } while (0)
#define STAMP_FLUSH(kid) do { } while (0)
DI unsigned blk_off(unsigned r, unsigned kc, unsigned KC) { return (((r >> 6) * KC + kc) << 10) + ((r & 63) << 4); }

template <int NT, int NC> DI int stage_of(int t, int r) {
    int c = t - 1;
    if (c >= NT - NC + r) c = t;
    else if ((c & (NC - 1)) == r) c += NC;
    return t == 0 ? r : c;
}
DI f32x2 finish_stats(const f32x2 (&p)[16]) {
    float m = 0.f;
#pragma unroll
    for (int i = 0; i < 16; ++i) m += p[i].x;
    m *= (1.f / 16.f);
    float q = 0.f;
#pragma unroll
    for (int i = 0; i < 16; ++i) { const float d = p[i].x - m; q += p[i].y + 64.f * d * d; }
    return (f32x2){m, __builtin_amdgcn_rsqf(q * (1.f / 1024.f) + LN_EPS)};
}

__global__ __launch_bounds__(256) void k_prologue(const bf16_t* __restrict__ x, const bf16_t* __restrict__ wqkv, const bf16_t* __restrict__ wg,
                                                  const bf16_t* __restrict__ wu, char* __restrict__ ws) {
    __shared__ __attribute__((aligned(16))) char sm[8 * 129 * 16];
    const int tid = threadIdx.x, lane = tid & 63, w = tid >> 6;
    if (blockIdx.x < 256) {
        const int r0 = blockIdx.x * 8;
#pragma unroll
        for (int i = 0; i < 2; ++i) {
            const int rl = w * 2 + i;
            const u32x4* src = (const u32x4*)(x + (size_t)(r0 + rl) * DM);
            const u32x4 a = src[lane], b = src[lane + 64];
            float v[16];
#pragma unroll
            for (int j = 0; j < 4; ++j) { v[2 * j] = bf_lo(a[j]); v[2 * j + 1] = bf_hi(a[j]); v[8 + 2 * j] = bf_lo(b[j]); v[8 + 2 * j + 1] = bf_hi(b[j]); }
            float s = 0.f;
#pragma unroll
            for (int j = 0; j < 16; ++j) s += v[j];
#pragma unroll
            for (int o = 32; o >= 1; o >>= 1) s += __shfl_xor(s, o);
            const float m = s * (1.f / 1024.f);
            float q = 0.f;
#pragma unroll
            for (int j = 0; j < 16; ++j) { v[j] -= m; q += v[j] * v[j]; }
#pragma unroll
            for (int o = 32; o >= 1; o >>= 1) q += __shfl_xor(q, o);
            const float rs = __builtin_amdgcn_rsqf(q * (1.f / 1024.f) + LN_EPS);
            u32x4 oa, ob;
#pragma unroll
            for (int j = 0; j < 4; ++j) { oa[j] = cvt_pk(v[2 * j] * rs, v[2 * j + 1] * rs); ob[j] = cvt_pk(v[8 + 2 * j] * rs, v[8 + 2 * j + 1] * rs); }
            *(u32x4*)(sm + (rl * 129 + lane) * 16) = oa;
            *(u32x4*)(sm + (rl * 129 + lane + 64) * 16) = ob;
        }
        __syncthreads();
#pragma unroll
        for (int i = 0; i < 4; ++i) {
            const int idx = tid + 256 * i, kc = idx >> 3, rl = idx & 7;
            *(u32x4*)(ws + WS_XN0 + blk_off(r0 + rl, kc, 128)) = *(const u32x4*)(sm + (rl * 129 + kc) * 16);
        }
    } else {
        const int u = blockIdx.x - 256, strip = u % 80, kp = u / 80;
        const bf16_t* W; int pitch, col0;
        if (strip < 24) { W = wqkv; pitch = 3 * DM; col0 = strip * 128; }
        else if (strip < 52) { W = wg; pitch = FF; col0 = (strip - 24) * 128; }
        else { W = wu; pitch = FF; col0 = (strip - 52) * 128; }
        const int cc = tid & 15, rg = tid >> 4;
        float s[8] = {0.f, 0.f, 0.f, 0.f, 0.f, 0.f, 0.f, 0.f};
        u32x4 v[8];
#pragma unroll
        for (int i = 0; i < 8; ++i) v[i] = *(const u32x4*)(W + (size_t)(kp * 128 + i * 16 + rg) * pitch + col0 + cc * 8);
#pragma unroll
        for (int i = 0; i < 8; ++i)
#pragma unroll
            for (int j = 0; j < 4; ++j) { s[2 * j] += bf_lo(v[i][j]); s[2 * j + 1] += bf_hi(v[i][j]); }
        float* red = (float*)sm;
#pragma unroll
        for (int j = 0; j < 8; ++j) red[rg * 128 + cc * 8 + j] = s[j];
        __syncthreads();
        if (tid < 128) {
            float t = 0.f;
#pragma unroll
            for (int i = 0; i < 16; ++i) t += red[i * 128 + tid];
            ((float*)(ws + WS_CS))[kp * CS_N + strip * 128 + tid] = t;
        }
    }
}

template <int NSEG_, int NSUB_, int S_, int KTOT_, int PITCH_, int SEGSTEP_, int NC_>
struct SplitCfg {
    static constexpr int NSEG = NSEG_, NSUB = NSUB_, S = S_, KTOT = KTOT_, PITCH = PITCH_, SEGSTEP = SEGSTEP_, NC = NC_;
    static constexpr int ABYTES = 2 * 4 * NSUB * 1024, BBYTES = NSUB * NSEG * 4096, STAGE = ABYTES + BBYTES;
    static constexpr int NT = KTOT / (32 * NSUB);
    static constexpr int KC = KTOT / 8;
    static constexpr int PA = 2 * NSUB, PB = NSUB * NSEG, P = PA + PB;
    static constexpr int RING = S * STAGE;
};

template <class C>
DI void split_loader(LAS char* lds, int lw, int lane, int cls, const char* A, unsigned a_bytes, unsigned a_base, const char* B, unsigned b_bytes, unsigned b_base STAMP_ARG) {
    const __amdgpu_buffer_rsrc_t ra = mk_rsrc(A, a_bytes), rb = mk_rsrc(B, b_bytes);
    const unsigned ldsb = (unsigned)(size_t)lds;
    const unsigned va = lane * 16;
    const unsigned rl = lane >> 3, sl = lane & 7;
    const unsigned vb = (8 * (rl >> 2) + (rl & 3)) * C::PITCH + (2 * ((sl >> 1) ^ ((rl >> 1) & 3)) + (sl & 1)) * 16;
    const unsigned a_src = a_base + (lw >> 1) * (C::KC * 1024) + (lw & 1) * (C::PA * 1024);
    const unsigned a_dst = (lw >> 1) * (4 * C::NSUB * 1024) + (lw & 1) * (C::PA * 1024);
    const unsigned b_src = b_base + (16 * (lw >> 1) + 4 * (lw & 1)) * C::PITCH;
    const unsigned b_dst = C::ABYTES + lw * 1024;
    auto issue = [&](int tt) {
        const unsigned t = stage_of<C::NT, C::NC>(tt, cls);
        const unsigned sb = ldsb + (unsigned)(tt % C::S) * C::STAGE;
#pragma unroll
        for (int p = 0; p < C::PA; ++p) dma16(sb + a_dst + p * 1024, va, ra, a_src + (t * 4 * C::NSUB + p) * 1024);
#pragma unroll
        for (int u = 0; u < C::NSUB; ++u)
#pragma unroll
            for (int g = 0; g < C::NSEG; ++g) dma16(sb + b_dst + (u * C::NSEG + g) * 4096, vb, rb, b_src + (t * C::NSUB + u) * 32 * C::PITCH + g * C::SEGSTEP);
    };
    static_assert(C::S == 3 || C::S == 4, "ring depth");
    issue(0); issue(1);
    STAMP(1);
    wait_vm<C::P>();
    STAMP(2);
    bar();
    issue(2);
    wait_vm<C::P>();
    bar();
    int nx = 3;
    if (C::S == 4) { issue(3); nx = 4; }
    issue(nx); ++nx;
    for (; nx < C::NT; ++nx) {
        wait_vm<C::P*(C::S - 2)>();
        bar();
        issue(nx);
    }
    wait_vm<C::P*(C::S - 2)>(); bar();
    if (C::S == 4) { wait_vm<C::P>(); bar(); }
    wait_vm<0>(); bar();
    STAMP(3);
}

template <class C> struct Frags { bf16x8 X[4]; bf16x8 W[C::NSEG][2]; };

template <class C, int TMASK>
struct Consumer {
    LAS char* lds; unsigned xa, wa[2];
    DI Consumer(LAS char* lds_, int cw, int lane) : lds(lds_) {
        const int wm = cw & 1, wn = cw >> 1;
        const int g = lane >> 4, fr = lane & 15, q = (lane & 15) >> 2, p = lane & 3;
        xa = wm * (4 * C::NSUB * 1024) + g * 1024 + fr * 16;
        const unsigned rho = 16 * (g >> 1) + 4 * (g & 1) + q, xr = 2 * (g & 1) + (q >> 1);
#pragma unroll
        for (int dt = 0; dt < 2; ++dt) wa[dt] = C::ABYTES + rho * 128 + (((2 * wn + dt) ^ xr) << 5) + (p >> 1) * 16 + (p & 1) * 8;
    }
    static constexpr int NR = 4 + 2 * C::NSEG;
    static constexpr int NM = 8 * C::NSEG;
    DI void load1(Frags<C>& f, unsigned sb, int u, int i) const {
        if (i >= 1 && i <= 4) f.X[i - 1] = lds_b128(lds + sb + xa + u * 4096 + (i - 1) * 256);
        else {
            const int k = i == 0 ? 0 : i - 4, s = k >> 1, dt = k & 1;
            LAS const char* pw = lds + sb + wa[dt] + (u * C::NSEG + s) * 4096;
            f.W[s][dt] = cat(lds_tr(pw), lds_tr(pw + 1024));
        }
    }
    DI void mma1(const Frags<C>& f, f32x4 (&acc)[C::NSEG][2][4], int i) const {
        const int s = i >> 3, dt = (i >> 2) & 1, mt = i & 3;
        if ((TMASK >> s) & 1) acc[s][dt][mt] = mfma(f.W[s][dt], f.X[mt], acc[s][dt][mt]);
        else acc[s][dt][mt] = mfma(f.X[mt], f.W[s][dt], acc[s][dt][mt]);
    }
};

template <class C, int TMASK>
DI void split_consumer(LAS char* lds, int cw, int lane, f32x4 (&acc)[C::NSEG][2][4] STAMP_ARG) {
    typedef Consumer<C, TMASK> K;
    const K k(lds, cw, lane);
    auto sub = [&](const Frags<C>& cur, Frags<C>& nxt, unsigned sb, int u, auto READ) {
#pragma unroll
        for (int i = 0; i < K::NM; ++i) {
            k.mma1(cur, acc, i);
            if constexpr (decltype(READ)::value) { if (i < K::NR) k.load1(nxt, sb, u, i); }
            SCHED();
        }
    };
    typedef std::integral_constant<bool, true> T_;
    typedef std::integral_constant<bool, false> F_;
    Frags<C> f0, f1;
    bar();
    STAMP(1);
#pragma unroll
    for (int i = 0; i < K::NR; ++i) k.load1(f0, 0, 0, i);
    wait_lgkm0();
    unsigned slot = 0;
    static_assert(C::NSUB == 2, "NSUB");
    for (int t = 0; t < C::NT - 1; ++t) {
        const unsigned sb = slot * C::STAGE;
        slot = (slot + 1 == C::S) ? 0 : slot + 1;
        sub(f0, f1, sb, 1, T_{});
        wait_lgkm0();
        bar();
        sub(f1, f0, slot * C::STAGE, 0, T_{});
        wait_lgkm0();
    }
    sub(f0, f1, slot * C::STAGE, 1, T_{});
    wait_lgkm0();
    sub(f1, f0, 0, 0, F_{});
    STAMP(2);
}

template <class C, int TMASK>
DI void split_consumer_stage(LAS char* lds, int cw, int lane, f32x4 (&acc)[C::NSEG][2][4] STAMP_ARG) {
    typedef Consumer<C, TMASK> K;
    const K k(lds, cw, lane);
    struct SF { Frags<C> u[C::NSUB]; };
    constexpr int NM = K::NM * C::NSUB, NR = K::NR * C::NSUB;
    auto step = [&](const SF& cur, SF& nxt, unsigned sb, auto READ, auto BARR) {
#pragma unroll
        for (int i = 0; i < NM; ++i) {
            k.mma1(cur.u[i / K::NM], acc, i % K::NM);
            if constexpr (decltype(READ)::value) {
                if (i < NR) k.load1(nxt.u[i / K::NR], sb, i / K::NR, i % K::NR);
            }
            SCHED();
        }
        if constexpr (decltype(BARR)::value) { wait_lgkm0(); bar(); }
    };
    typedef std::integral_constant<bool, true> T_;
    typedef std::integral_constant<bool, false> F_;
    SF f0, f1;
    bar();
    STAMP(1);
#pragma unroll
    for (int i = 0; i < NR; ++i) k.load1(f0.u[i / K::NR], 0, i / K::NR, i % K::NR);
    wait_lgkm0();
    bar();
    static_assert(C::NT % 2 == 0 && C::S == 4, "stage loop");
    for (int t = 0; t < C::NT - 2; t += 2) {
        step(f0, f1, ((t + 1) & 3) * C::STAGE, T_{}, T_{});
        step(f1, f0, ((t + 2) & 3) * C::STAGE, T_{}, T_{});
    }
    step(f0, f1, ((C::NT - 1) & 3) * C::STAGE, T_{}, F_{});
    step(f1, f0, 0, F_{}, F_{});
    STAMP(2);
}

typedef SplitCfg<3, 2, 3, DM, 3 * DM * 2, 2048, 4> CfgQKV;
constexpr int QKV_LDS = CfgQKV::RING + 4096;

template <bool LNFUSE>
__global__ __launch_bounds__(512) void k_qkv(const char* __restrict__ A, const bf16_t* __restrict__ wqkv, char* __restrict__ ws, size_t stats_off) {
    typedef CfgQKV C;
    extern __shared__ __attribute__((aligned(16))) char smem[];
    LAS char* lds = (LAS char*)smem;
    const int tid = threadIdx.x, lane = tid & 63, w = __builtin_amdgcn_readfirstlane(tid >> 6);
    const int xcd = blockIdx.x & 7, j = blockIdx.x >> 3;
    const int pm = 8 * (xcd & 1) + (j & 7), hp = 4 * (xcd >> 1) + (j >> 3);
    STAMP_DECL; STAMP(0);
    if (w >= 4) {
        split_loader<C>(lds, w - 4, lane, (j + (j >> 3)) & 3, A, SEQ * DM * 2, pm * 2 * (C::KC * 1024), (const char*)wqkv, DM * 3 * DM * 2, hp * 128 STAMP_PASS);
        STAMP_FLUSH(0);
        bar();
        bar();
        return;
    }
    LAS f32x2* st = (LAS f32x2*)(lds + C::RING);
    LAS float* cs = (LAS float*)(lds + C::RING + 1024);
    if (LNFUSE) {
        if (tid < 128) {
            const f32x2* sp = (const f32x2*)(ws + stats_off) + pm * 128 + tid;
            f32x2 pp[16];
#pragma unroll
            for (int i = 0; i < 16; ++i) pp[i] = sp[i * SEQ];
            st[tid] = finish_stats(pp);
        }
        if (tid < 192) {
            const float* cp = (const float*)(ws + WS_CS) + (tid >> 6) * DM + hp * 64 + (tid & 63);
            float s = 0.f;
#pragma unroll
            for (int i = 0; i < CS_KP; ++i) s += cp[i * CS_N];
            cs[tid] = s;
        }
        wait_lgkm0();
    }
    f32x4 acc[3][2][4];
#pragma unroll
    for (int s = 0; s < 3; ++s)
#pragma unroll
        for (int d = 0; d < 2; ++d)
#pragma unroll
            for (int m = 0; m < 4; ++m) acc[s][d][m] = (f32x4){0.f, 0.f, 0.f, 0.f};
    split_consumer<C, 1>(lds, w, lane, acc STAMP_PASS);

    const int wm = w & 1, hh = w >> 1, g = lane >> 4, fr = lane & 15;
    const int head = hp * 2 + hh;
#pragma unroll
    for (int mt = 0; mt < 4; ++mt) {
        float mean = 0.f, rstd = 1.f;
        if (LNFUSE) { const f32x2 s = st[wm * 64 + 16 * mt + fr]; mean = s.x; rstd = s.y; }
        unsigned pk[2][2];
#pragma unroll
        for (int dt = 0; dt < 2; ++dt) {
            float v[4];
#pragma unroll
            for (int i = 0; i < 4; ++i) {
                float t = acc[0][dt][mt][i];
                if (LNFUSE) t = rstd * (t - mean * cs[hh * 32 + 16 * dt + 4 * g + i]);
                v[i] = phi_f(t);
            }
            pk[dt][0] = cvt_pk(v[0], v[1]); pk[dt][1] = cvt_pk(v[2], v[3]);
        }
        swap16(pk[0][0], pk[1][0]); swap16(pk[0][1], pk[1][1]);
        const int ch = ((g & 1) << 1) | (g >> 1);
        const u32x4 o = {pk[0][0], pk[0][1], pk[1][0], pk[1][1]};
        *(u32x4*)(ws + WS_PQ + blk_off(pm * 128 + wm * 64 + 16 * mt + fr, head * 4 + ch, 128)) = o;
    }
    bf16x8 kf[2][2], vf[2][2];
#pragma unroll
    for (int dt = 0; dt < 2; ++dt) {
        float ck = 0.f, cv = 0.f;
        if (LNFUSE) { ck = cs[64 + hh * 32 + 16 * dt + fr]; cv = cs[128 + hh * 32 + 16 * dt + fr]; }
#pragma unroll
        for (int sp = 0; sp < 2; ++sp) {
            u32x4 ku, vu;
#pragma unroll
            for (int h2 = 0; h2 < 2; ++h2) {
                const int mt = 2 * sp + h2;
                float a[4], b[4];
#pragma unroll
                for (int i = 0; i < 4; ++i) {
                    float tk = acc[1][dt][mt][i], tv = acc[2][dt][mt][i];
                    if (LNFUSE) {
                        const f32x2 s = st[wm * 64 + 16 * mt + 4 * g + i];
                        tk = s.y * (tk - s.x * ck); tv = s.y * (tv - s.x * cv);
                    }
                    a[i] = phi_f(tk); b[i] = tv;
                }
                ku[2 * h2] = cvt_pk(a[0], a[1]); ku[2 * h2 + 1] = cvt_pk(a[2], a[3]);
                vu[2 * h2] = cvt_pk(b[0], b[1]); vu[2 * h2 + 1] = cvt_pk(b[2], b[3]);
            }
            kf[dt][sp] = __builtin_bit_cast(bf16x8, ku); vf[dt][sp] = __builtin_bit_cast(bf16x8, vu);
        }
    }
    const bf16x8 ones = {0x3F80, 0x3F80, 0x3F80, 0x3F80, 0x3F80, 0x3F80, 0x3F80, 0x3F80};
    f32x4 kv[2][2], z[2];
#pragma unroll
    for (int dt = 0; dt < 2; ++dt) {
        z[dt] = mfma(kf[dt][0], ones, (f32x4){0.f, 0.f, 0.f, 0.f});
        z[dt] = mfma(kf[dt][1], ones, z[dt]);
#pragma unroll
        for (int et = 0; et < 2; ++et) {
            kv[dt][et] = mfma(kf[dt][0], vf[et][0], (f32x4){0.f, 0.f, 0.f, 0.f});
            kv[dt][et] = mfma(kf[dt][1], vf[et][1], kv[dt][et]);
        }
    }
    bar();
    LAS f32x4* red = (LAS f32x4*)(lds + hh * 8192);
    if (wm == 1) {
#pragma unroll
        for (int dt = 0; dt < 2; ++dt) {
#pragma unroll
            for (int et = 0; et < 2; ++et) red[(dt * 2 + et) * 64 + lane] = kv[dt][et];
            red[(4 + dt) * 64 + lane] = z[dt];
        }
        wait_lgkm0();
    }
    bar();
    if (wm == 0) {
        float* dst = (float*)(ws + WS_KVP) + (size_t)(pm * NH + head) * KVP_STRIDE;
#pragma unroll
        for (int dt = 0; dt < 2; ++dt) {
#pragma unroll
            for (int et = 0; et < 2; ++et) *(f32x4*)(dst + ((dt * 2 + et) * 64 + lane) * 4) = kv[dt][et] + red[(dt * 2 + et) * 64 + lane];
            const f32x4 zz = z[dt] + red[(4 + dt) * 64 + lane];
            if (fr == 0) *(f32x4*)(dst + 1024 + 16 * dt + 4 * g) = zz;
        }
    }
    STAMP(3); STAMP_FLUSH(0);
}

__global__ __launch_bounds__(256) void k_attn(char* __restrict__ ws) {
    __shared__ __attribute__((aligned(16))) char sm[32 * 80 + 64];
    const int tid = threadIdx.x, lane = tid & 63, w = tid >> 6, g = lane >> 4, fr = lane & 15;
    const int xcd = blockIdx.x & 7, j = blockIdx.x >> 3;
    const int head = 4 * xcd + (j & 3), pb = j >> 2;
    bf16x8 qf[4];
#pragma unroll
    for (int mt = 0; mt < 4; ++mt) qf[mt] = *(const bf16x8*)(ws + WS_PQ + blk_off(pb * 256 + w * 64 + 16 * mt + fr, head * 4 + g, 128));
    const float* src = (const float*)(ws + WS_KVP) + (size_t)head * KVP_STRIDE;
    f32x4 s = {0.f, 0.f, 0.f, 0.f};
#pragma unroll
    for (int i = 0; i < 16; ++i) s += *(const f32x4*)(src + (size_t)i * NH * KVP_STRIDE + tid * 4);
    {
        const int dt = w >> 1, et = w & 1;
        u32x2 pk = {cvt_pk(s[0], s[1]), cvt_pk(s[2], s[3])};
        *(u32x2*)(sm + (16 * et + fr) * 80 + (16 * dt + 4 * g) * 2) = pk;
    }
    if (tid < 32) {
        float zz = 0.f;
#pragma unroll
        for (int i = 0; i < 16; ++i) zz += src[(size_t)i * NH * KVP_STRIDE + 1024 + tid];
        *(bf16_t*)(sm + 32 * 80 + tid * 2) = (bf16_t)(cvt_pk(zz, 0.f) & 0xffff);
    }
    __syncthreads();
    bf16x8 kf[2];
#pragma unroll
    for (int et = 0; et < 2; ++et) kf[et] = *(const bf16x8*)(sm + (16 * et + fr) * 80 + g * 16);
    const bf16x8 zf = *(const bf16x8*)(sm + 32 * 80 + g * 16);
    const f32x4 zero = {0.f, 0.f, 0.f, 0.f};
#pragma unroll
    for (int mt = 0; mt < 4; ++mt) {
        const f32x4 den = mfma(zf, qf[mt], zero);
        const f32x4 n0 = mfma(kf[0], qf[mt], zero), n1 = mfma(kf[1], qf[mt], zero);
        const float r = __builtin_amdgcn_rcpf(den[0] + 1e-6f);
        unsigned a0 = cvt_pk(n0[0] * r, n0[1] * r), a1 = cvt_pk(n0[2] * r, n0[3] * r);
        unsigned b0 = cvt_pk(n1[0] * r, n1[1] * r), b1 = cvt_pk(n1[2] * r, n1[3] * r);
        swap16(a0, b0); swap16(a1, b1);
        const int ch = ((g & 1) << 1) | (g >> 1);
        *(u32x4*)(ws + WS_O + blk_off(pb * 256 + w * 64 + 16 * mt + fr, head * 4 + ch, 128)) = (u32x4){a0, a1, b0, b1};
    }
}

typedef SplitCfg<1, 2, 4, DM, DM * 2, 0, 4> CfgWO;
typedef SplitCfg<1, 2, 4, FF, DM * 2, 0, 4> CfgDN;

template <class C, bool RES_RM, bool OUT_RM>
__global__ __launch_bounds__(512) void k_n1024(const char* __restrict__ A, const bf16_t* __restrict__ W, const char* __restrict__ res, char* __restrict__ out,
                                               f32x2* __restrict__ stats) {
    extern __shared__ __attribute__((aligned(16))) char smem[];
    LAS char* lds = (LAS char*)smem;
    const int tid = threadIdx.x, lane = tid & 63, w = __builtin_amdgcn_readfirstlane(tid >> 6);
    const int xcd = blockIdx.x & 7, j = blockIdx.x >> 3;
    const int pm = 4 * (xcd & 3) + (j & 3), pn = 8 * (xcd >> 2) + (j >> 2);
    STAMP_DECL; STAMP(0);
    if (w >= 4) {
        split_loader<C>(lds, w - 4, lane, (j + (j >> 2)) & 3, A, SEQ * C::KTOT * 2, pm * 2 * (C::KC * 1024), (const char*)W, C::KTOT * DM * 2, pn * 128 STAMP_PASS);
        STAMP_FLUSH(C::KTOT == DM ? 1 : 2);
        if (!OUT_RM) { bar(); bar(); }
        return;
    }
    const int wm = w & 1, wn = w >> 1, g = lane >> 4, fr = lane & 15;
    const int ch = ((g & 1) << 1) | (g >> 1);
    const int kc = pn * 8 + wn * 4 + ch;
    u32x4 rv[4];
#pragma unroll
    for (int mt = 0; mt < 4; ++mt) {
        const int row = pm * 128 + wm * 64 + 16 * mt + fr;
        rv[mt] = RES_RM ? *(const u32x4*)(res + (size_t)row * (DM * 2) + kc * 16) : *(const u32x4*)(res + blk_off(row, kc, 128));
    }
    f32x4 acc[1][2][4];
#pragma unroll
    for (int d = 0; d < 2; ++d)
#pragma unroll
        for (int m = 0; m < 4; ++m) acc[0][d][m] = (f32x4){0.f, 0.f, 0.f, 0.f};
    split_consumer_stage<C, 1>(lds, w, lane, acc STAMP_PASS);

    f32x2 part[4];
#pragma unroll
    for (int mt = 0; mt < 4; ++mt) {
        float v[8];
#pragma unroll
        for (int i = 0; i < 4; ++i) { float a = acc[0][0][mt][i], b = acc[0][1][mt][i]; swap16f(a, b); v[i] = a; v[4 + i] = b; }
        u32x4 o;
#pragma unroll
        for (int i = 0; i < 4; ++i) o[i] = cvt_pk(v[2 * i] + bf_lo(rv[mt][i]), v[2 * i + 1] + bf_hi(rv[mt][i]));
        const int row = pm * 128 + wm * 64 + 16 * mt + fr;
        if (OUT_RM) *(u32x4*)(out + (size_t)row * (DM * 2) + kc * 16) = o;
        else {
            *(u32x4*)(out + blk_off(row, kc, 128)) = o;
            float s = 0.f;
#pragma unroll
            for (int i = 0; i < 4; ++i) { v[2 * i] = bf_lo(o[i]); v[2 * i + 1] = bf_hi(o[i]); s += v[2 * i] + v[2 * i + 1]; }
            const float m = xrow_sum(s) * (1.f / 32.f);
            float q = 0.f;
#pragma unroll
            for (int i = 0; i < 8; ++i) q += (v[i] - m) * (v[i] - m);
            part[mt] = (f32x2){m, xrow_sum(q)};
        }
    }
    if (!OUT_RM) {
        bar();
        LAS f32x2* sp = (LAS f32x2*)lds;
        if (wn == 1 && g == 0) {
#pragma unroll
            for (int mt = 0; mt < 4; ++mt) sp[wm * 64 + 16 * mt + fr] = part[mt];
        }
        wait_lgkm0();
        bar();
        if (wn == 0 && g == 0) {
#pragma unroll
            for (int mt = 0; mt < 4; ++mt) {
                const f32x2 o = sp[wm * 64 + 16 * mt + fr];
                const float d = part[mt].x - o.x;
                stats[pn * SEQ + pm * 128 + wm * 64 + 16 * mt + fr] = (f32x2){0.5f * (part[mt].x + o.x), part[mt].y + o.y + 16.f * d * d};
            }
        }
    }
    STAMP(3); STAMP_FLUSH(C::KTOT == DM ? 1 : 2);
}

#define GU_LEAD 3
#define GU_NC 4
constexpr int GU_S = 4;
constexpr int GU_GOFF = 16384, GU_UOFF = 16384 + 7168 + 16, GU_STAGE = GU_UOFF + 7168;
constexpr int GU_RING = GU_S * GU_STAGE;
constexpr int GU_DUMMY = GU_RING, GU_STATS = GU_DUMMY + 1024, GU_CS = GU_STATS + 2048, GU_LDS = GU_CS + 1024;
struct GuFrags { bf16x8 X[4]; bf16x8 W[7]; };

__global__ __launch_bounds__(512) void k_gateup(const char* __restrict__ A, const bf16_t* __restrict__ wg, const bf16_t* __restrict__ wu, char* __restrict__ ws,
                                                size_t stats_off) {
    extern __shared__ __attribute__((aligned(16))) char smem[];
    LAS char* lds = (LAS char*)smem;
    const int tid = threadIdx.x, lane = tid & 63, w = __builtin_amdgcn_readfirstlane(tid >> 6);
    const int xcd = blockIdx.x & 7, jj = blockIdx.x >> 3;
    const int pm = 4 * (xcd & 1) + (jj & 3), pn = 8 * (xcd >> 1) + (jj >> 2);
    const int wr = w >> 1, wc = w & 1, g = lane >> 4, fr = lane & 15, q = fr >> 2, p = lane & 3;

    STAMP_DECL; STAMP(0);
    f32x2 sp[8]; float cp[8];
    {
        const char* sb = ws + stats_off + ((size_t)((tid & 1) * 8) * SEQ + pm * 256 + (tid >> 1)) * 8;
#pragma unroll
        for (int i = 0; i < 8; ++i) asm volatile("global_load_dwordx2 %0, %1, off" : "=v"(sp[i]) : "v"(sb + (size_t)i * SEQ * 8) : "memory");
        const int c = tid < 224 ? tid : 0;
        const char* cb = ws + WS_CS + (size_t)(3 * DM + (c >= 112 ? FF + c - 112 : c) + pn * 112) * 4;
#pragma unroll
        for (int i = 0; i < 8; ++i) asm volatile("global_load_dword %0, %1, off" : "=v"(cp[i]) : "v"(cb + (size_t)i * CS_N * 4) : "memory");
    }
    const __amdgpu_buffer_rsrc_t ra = mk_rsrc(A, SEQ * DM * 2);
    const __amdgpu_buffer_rsrc_t rg = mk_rsrc(wg, w <= 6 ? DM * FF * 2 : 0), ru = mk_rsrc(wu, w >= 1 ? DM * FF * 2 : 0);
    const unsigned ldsb = (unsigned)(size_t)lds;
    const unsigned va = lane * 16;
    auto bpat = [&](int piece) { const unsigned i = 64 * piece + lane, rho = i / 14, c = i % 14; const unsigned k = (rho & 0x13) | ((rho & 8) >> 1) | ((rho & 4) << 1); return k * (FF * 2) + c * 16; };
    const unsigned vg = bpat(w <= 6 ? w : 0), vu = bpat(w >= 1 ? w - 1 : 0);
    const unsigned a_src = (pm * 4 + (w >> 1)) * (128 * 1024) + (w & 1) * 2048, a_dst = (w >> 1) * 4096 + (w & 1) * 2048;
    const unsigned g_dst0 = w <= 6 ? GU_GOFF + w * 1024 : GU_DUMMY, g_str = w <= 6 ? GU_STAGE : 0;
    const unsigned u_dst0 = w >= 1 ? GU_UOFF + (w - 1) * 1024 : GU_DUMMY, u_str = w >= 1 ? GU_STAGE : 0;
    const unsigned b_src = pn * 224;
    const int cls = ((jj & 3) + (jj >> 2)) & (GU_NC - 1);
    auto issue1 = [&](int ks, unsigned slot, int i) {
        if (i < 2) dma16(ldsb + slot * GU_STAGE + a_dst + i * 1024, va, ra, a_src + (ks * 4 + i) * 1024);
        else if (i == 2) dma16(ldsb + g_dst0 + slot * g_str, vg, rg, b_src + ks * (32 * FF * 2));
        else dma16(ldsb + u_dst0 + slot * u_str, vu, ru, b_src + ks * (32 * FF * 2));
    };
    auto issue = [&](int t) {
        const int ks = stage_of<32, GU_NC>(t, cls);
#pragma unroll
        for (int i = 0; i < 4; ++i) issue1(ks, t & 3, i);
    };
    issue(0); issue(1);

    const unsigned xa = wr * 4096 + g * 1024 + fr * 16;
    const unsigned rho0 = 16 * (g >> 1) + 4 * (g & 1) + q;
    const unsigned wa = (rho0 * 14 + 7 * wc + (p >> 1)) * 16 + (p & 1) * 8;
    const unsigned wm_ = (rho0 * 14 + 7 * wc + 6) * 16 + (p & 1) * 8 + ((p >> 1) ? GU_UOFF : GU_GOFF);
    auto load1 = [&](GuFrags& f, unsigned sb, int i) {
        if (i < 4) f.X[i] = lds_b128(lds + sb + xa + i * 256);
        else {
            const int T = i - 4;
            LAS const char* pw = T < 3 ? lds + sb + wa + GU_GOFF + T * 32 : (T < 6 ? lds + sb + wa + GU_UOFF + (T - 3) * 32 : lds + sb + wm_);
            f.W[T] = cat(lds_tr(pw), lds_tr(pw + 1792));
        }
    };

    wait_vm<4>();
    asm volatile("" : "+v"(sp[0]), "+v"(sp[1]), "+v"(sp[2]), "+v"(sp[3]), "+v"(sp[4]), "+v"(sp[5]), "+v"(sp[6]), "+v"(sp[7]));
    asm volatile("" : "+v"(cp[0]), "+v"(cp[1]), "+v"(cp[2]), "+v"(cp[3]), "+v"(cp[4]), "+v"(cp[5]), "+v"(cp[6]), "+v"(cp[7]));
    {
        float m = 0.f;
#pragma unroll
        for (int i = 0; i < 8; ++i) m += sp[i].x;
        m += __shfl_xor(m, 1);
        m *= (1.f / 16.f);
        float qq = 0.f;
#pragma unroll
        for (int i = 0; i < 8; ++i) { const float d = sp[i].x - m; qq += sp[i].y + 64.f * d * d; }
        qq += __shfl_xor(qq, 1);
        if ((tid & 1) == 0) ((LAS f32x2*)(lds + GU_STATS))[tid >> 1] = (f32x2){m, __builtin_amdgcn_rsqf(qq * (1.f / 1024.f) + LN_EPS)};
        float c = 0.f;
#pragma unroll
        for (int i = 0; i < 8; ++i) c += cp[i];
        if (tid < 224) ((LAS float*)(lds + GU_CS))[tid] = c;
    }
    wait_lgkm0();
    bar();
    GuFrags f0, f1;
#pragma unroll
    for (int i = 0; i < 11; ++i) load1(f0, 0, i);
    issue(2);
    if (GU_LEAD == 4) issue(3);
    wait_vm<4 * (GU_LEAD - 2)>();
    wait_lgkm0();
    bar();
    STAMP(1);

    f32x4 acc[7][4];
#pragma unroll
    for (int T = 0; T < 7; ++T)
#pragma unroll
        for (int m = 0; m < 4; ++m) acc[T][m] = (f32x4){0.f, 0.f, 0.f, 0.f};

    auto step = [&](const GuFrags& cur, GuFrags& nxt, int t, auto DMA, auto READ, auto VM) {
        const unsigned nsb = ((t + 1) & (GU_S - 1)) * GU_STAGE;
        const unsigned dslot = (t + GU_LEAD) & (GU_S - 1);
        const int ks = stage_of<32, GU_NC>(t + GU_LEAD, cls);
#pragma unroll
        for (int i = 0; i < 28; ++i) {
            const int T = i >> 2, m = i & 3;
            acc[T][m] = mfma(cur.W[T], cur.X[m], acc[T][m]);
            if constexpr (decltype(READ)::value) { if (i < 22 && (i & 1) == 0) load1(nxt, nsb, i >> 1); }
            if constexpr (decltype(DMA)::value) { if ((i == 1 || i == 7 || i == 13 || i == 19)
                ) issue1(ks, dslot, (i - 1) / 6); }
            SCHED();
        }
        if constexpr (decltype(READ)::value) {
            wait_vm<decltype(VM)::value>();
            wait_lgkm0();
            bar();
        }
    };
    typedef std::integral_constant<bool, true> T_;
    typedef std::integral_constant<bool, false> F_;
    for (int t = 0; t < 28; t += 2) {
        step(f0, f1, t, T_{}, T_{}, std::integral_constant<int, 4 * (GU_LEAD - 2)>{});
        step(f1, f0, t + 1, T_{}, T_{}, std::integral_constant<int, 4 * (GU_LEAD - 2)>{});
    }
    if (GU_LEAD == 3) step(f0, f1, 28, T_{}, T_{}, std::integral_constant<int, 4>{});
    else step(f0, f1, 28, F_{}, T_{}, std::integral_constant<int, 4>{});
    step(f1, f0, 29, F_{}, T_{}, std::integral_constant<int, 0>{});
    step(f0, f1, 30, F_{}, T_{}, std::integral_constant<int, 0>{});
    step(f1, f0, 31, F_{}, F_{}, std::integral_constant<int, 0>{});

    STAMP(2);
    const LAS float* cs = (const LAS float*)(lds + GU_CS);
    const LAS f32x2* st = (const LAS f32x2*)(lds + GU_STATS);
    auto swiglu = [&](float ga, float up) { return ga * up * __builtin_amdgcn_rcpf(1.f + __builtin_amdgcn_exp2f(ga * -1.44269504088896341f)); };
    float cg[3][4], cu[3][4], cm[4];
#pragma unroll
    for (int T = 0; T < 3; ++T)
#pragma unroll
        for (int i = 0; i < 4; ++i) { cg[T][i] = cs[56 * wc + 16 * T + 4 * g + i]; cu[T][i] = cs[112 + 56 * wc + 16 * T + 4 * g + i]; }
#pragma unroll
    for (int i = 0; i < 4; ++i) cm[i] = cs[(g >> 1) * 112 + 56 * wc + 48 + 4 * (g & 1) + i];
    char* hb = ws + WS_H;
    const int kc0 = pn * 14 + wc * 7;
    const int ch = ((g & 1) << 1) | (g >> 1);
    unsigned P2[4][2], Mx[4];
#pragma unroll
    for (int m = 0; m < 4; ++m) {
        const f32x2 s = st[wr * 64 + 16 * m + fr];
        const float mr = s.x * s.y;
        unsigned P[3][2];
#pragma unroll
        for (int T = 0; T < 3; ++T) {
            float h[4];
#pragma unroll
            for (int i = 0; i < 4; ++i) h[i] = swiglu(s.y * acc[T][m][i] - mr * cg[T][i], s.y * acc[T + 3][m][i] - mr * cu[T][i]);
            P[T][0] = cvt_pk(h[0], h[1]); P[T][1] = cvt_pk(h[2], h[3]);
        }
        {
            float v[4];
#pragma unroll
            for (int i = 0; i < 4; ++i) v[i] = s.y * acc[6][m][i] - mr * cm[i];
            swap32f(v[0], v[2]); swap32f(v[1], v[3]);
            Mx[m] = cvt_pk(swiglu(v[0], v[2]), swiglu(v[1], v[3]));
        }
        swap16(P[0][0], P[1][0]); swap16(P[0][1], P[1][1]);
        {
            const int c2 = kc0 + 2 * (g & 1) + (g >> 1);
            *(u32x4*)(hb + blk_off(pm * 256 + wr * 64 + 16 * m + fr, c2, FF / 8)) = (u32x4){P[0][0], P[0][1], P[1][0], P[1][1]};
        }
        P2[m][0] = P[2][0]; P2[m][1] = P[2][1];
        if (m & 1) {
            swap16(P2[m - 1][0], P2[m][0]); swap16(P2[m - 1][1], P2[m][1]);
            const int row = pm * 256 + wr * 64 + 16 * (m - 1 + (g & 1)) + fr;
            *(u32x4*)(hb + blk_off(row, kc0 + 4 + (g >> 1), FF / 8)) = (u32x4){P2[m - 1][0], P2[m - 1][1], P2[m][0], P2[m][1]};
        }
    }
    {
        swap32(Mx[0], Mx[2]); swap32(Mx[1], Mx[3]);
        swap16(Mx[0], Mx[1]); swap16(Mx[2], Mx[3]);
        const int row = pm * 256 + wr * 64 + 16 * g + fr;
        *(u32x4*)(hb + blk_off(row, kc0 + 6, FF / 8)) = (u32x4){Mx[0], Mx[2], Mx[1], Mx[3]};
    }
    (void)ch;
    STAMP(3); STAMP_FLUSH(3);
}

}

extern "C" void kernel_launch(void* const* d_in, const int* in_sizes, int n_in, void* d_out, int out_size, void* d_ws, size_t ws_size, hipStream_t stream) {
    const bf16_t* x = (const bf16_t*)d_in[0];
    const bf16_t* wqkv = (const bf16_t*)d_in[1];
    const bf16_t* wo = (const bf16_t*)d_in[2];
    const bf16_t* wg = (const bf16_t*)d_in[4];
    const bf16_t* wu = (const bf16_t*)d_in[5];
    const bf16_t* wd = (const bf16_t*)d_in[6];
    char* ws = (char*)d_ws;
    (void)hipFuncSetAttribute((const void*)k_qkv<false>, hipFuncAttributeMaxDynamicSharedMemorySize, QKV_LDS);
    (void)hipFuncSetAttribute((const void*)k_qkv<true>, hipFuncAttributeMaxDynamicSharedMemorySize, QKV_LDS);
    (void)hipFuncSetAttribute((const void*)k_n1024<CfgWO, true, false>, hipFuncAttributeMaxDynamicSharedMemorySize, CfgWO::RING);
    (void)hipFuncSetAttribute((const void*)k_n1024<CfgWO, false, false>, hipFuncAttributeMaxDynamicSharedMemorySize, CfgWO::RING);
    (void)hipFuncSetAttribute((const void*)k_n1024<CfgDN, false, false>, hipFuncAttributeMaxDynamicSharedMemorySize, CfgDN::RING);
    (void)hipFuncSetAttribute((const void*)k_n1024<CfgDN, false, true>, hipFuncAttributeMaxDynamicSharedMemorySize, CfgDN::RING);
    (void)hipFuncSetAttribute((const void*)k_gateup, hipFuncAttributeMaxDynamicSharedMemorySize, GU_LDS);

    k_prologue<<<256 + 640, 256, 0, stream>>>(x, wqkv, wg, wu, ws);
    k_qkv<false><<<256, 512, QKV_LDS, stream>>>(ws + WS_XN0, wqkv, ws, WS_ST2);
    k_attn<<<256, 256, 0, stream>>>(ws);
    k_n1024<CfgWO, true, false><<<256, 512, CfgWO::RING, stream>>>(ws + WS_O, wo, (const char*)x, ws + WS_X1, (f32x2*)(ws + WS_ST1));
    k_gateup<<<256, 512, GU_LDS, stream>>>(ws + WS_X1, wg, wu, ws, WS_ST1);
    k_n1024<CfgDN, false, false><<<256, 512, CfgDN::RING, stream>>>(ws + WS_H, wd, ws + WS_X1, ws + WS_X2, (f32x2*)(ws + WS_ST2));
    k_qkv<true><<<256, 512, QKV_LDS, stream>>>(ws + WS_X2, wqkv, ws, WS_ST2);
    k_attn<<<256, 256, 0, stream>>>(ws);
    k_n1024<CfgWO, false, false><<<256, 512, CfgWO::RING, stream>>>(ws + WS_O, wo, ws + WS_X2, ws + WS_X1, (f32x2*)(ws + WS_ST1));
    k_gateup<<<256, 512, GU_LDS, stream>>>(ws + WS_X1, wg, wu, ws, WS_ST1);
    k_n1024<CfgDN, false, true><<<256, 512, CfgDN::RING, stream>>>(ws + WS_H, wd, ws + WS_X1, (char*)d_out, nullptr);
}
```

```cpp
#include <hip/hip_runtime.h>
#include <hip/hip_bf16.h>
#include <stdint.h>
#include <type_traits>

namespace {

typedef unsigned short bf16_t;
typedef unsigned long long u64;
typedef short bf16x8 __attribute__((ext_vector_type(8)));
typedef short bf16x4 __attribute__((ext_vector_type(4)));
typedef __bf16 bf16x2n __attribute__((ext_vector_type(2)));
typedef float f32x4 __attribute__((ext_vector_type(4)));
typedef float f32x2 __attribute__((ext_vector_type(2)));
typedef unsigned u32x4 __attribute__((ext_vector_type(4)));
typedef unsigned u32x2 __attribute__((ext_vector_type(2)));
#define LAS __attribute__((address_space(3)))
#define DI __device__ __forceinline__

constexpr int SEQ = 2048, DM = 1024, FF = 3584, NH = 32;
constexpr float LN_EPS = 1e-5f;

constexpr size_t MB = 1u << 20;
constexpr size_t WS_XN0 = 0;
constexpr size_t WS_PQ = 4 * MB;
constexpr size_t WS_O = 12 * MB;
constexpr size_t WS_X1 = 20 * MB;
constexpr size_t WS_X2 = 28 * MB;
constexpr size_t WS_H = 32 * MB;
constexpr size_t WS_KVP = 64 * MB;
constexpr size_t WS_ST1 = 72 * MB;
constexpr size_t WS_ST2 = 74 * MB;
constexpr size_t WS_CSQ = 75 * MB;
constexpr size_t WS_CSG = 76 * MB;
constexpr size_t WS_EX = 80 * MB;
constexpr size_t WS_FLAG = 96 * MB;
constexpr size_t WS_EPOCH = WS_FLAG + 16 * 256 * 8 + 1024;
constexpr int KVP_STRIDE = 1056;
enum { F_XN = 0, F_QKV = 1, F_ATT = 2, F_WO = 3, F_GU = 4, F_EX = 5, F_DN = 6, F_PER_LAYER = 6 };

DI unsigned cvt_pk(float lo, float hi) { f32x2 v = {lo, hi}; bf16x2n b = __builtin_convertvector(v, bf16x2n); return __builtin_bit_cast(unsigned, b); }
DI unsigned cvt_pk2(f32x2 v) { bf16x2n b = __builtin_convertvector(v, bf16x2n); return __builtin_bit_cast(unsigned, b); }
DI float bf_lo(unsigned u) { return __uint_as_float(u << 16); }
DI float bf_hi(unsigned u) { return __uint_as_float(u & 0xffff0000u); }
DI f32x2 phi2(f32x2 t) {
    const f32x2 u = t * 1.44269504088896341f;
    const f32x2 e = {__builtin_amdgcn_exp2f(__builtin_fminf(u.x, 0.f)), __builtin_amdgcn_exp2f(__builtin_fminf(u.y, 0.f))};
    const f32x2 m = {__builtin_fmaxf(t.x, 0.f), __builtin_fmaxf(t.y, 0.f)};
    return (m + e) + 1e-6f;
}
DI void swap16(unsigned& a, unsigned& b) { auto r = __builtin_amdgcn_permlane16_swap(a, b, false, false); a = r[0]; b = r[1]; }
DI void swap32(unsigned& a, unsigned& b) { auto r = __builtin_amdgcn_permlane32_swap(a, b, false, false); a = r[0]; b = r[1]; }
DI void swap16f(float& a, float& b) { unsigned x = __float_as_uint(a), y = __float_as_uint(b); swap16(x, y); a = __uint_as_float(x); b = __uint_as_float(y); }
DI void swap32f(float& a, float& b) { unsigned x = __float_as_uint(a), y = __float_as_uint(b); swap32(x, y); a = __uint_as_float(x); b = __uint_as_float(y); }
DI float xrow_sum(float x) {
    float a = x, b = x; swap16f(a, b); x = a + b;
    a = x; b = x; swap32f(a, b); return a + b;
}
template <int CTRL> DI float dpp(float x) { return __builtin_bit_cast(float, __builtin_amdgcn_update_dpp(0, __builtin_bit_cast(int, x), CTRL, 0xf, 0xf, true)); }
DI float sum4(float x) { x += dpp<0xB1>(x); x += dpp<0x4E>(x); return x; }
DI float sum8(float x) { x = sum4(x); x += dpp<0x141>(x); return x; }
template <bool SC1> DI void dma16(unsigned m0v, unsigned voff, __amdgpu_buffer_rsrc_t rs, unsigned soff) {
    if (SC1) asm volatile("s_mov_b32 m0, %0\n\ts_nop 0\n\tbuffer_load_dwordx4 %1, %2, %3 offen sc1 lds" ::"s"(m0v), "v"(voff), "s"(rs), "s"(soff) : "memory");
    else asm volatile("s_mov_b32 m0, %0\n\ts_nop 0\n\tbuffer_load_dwordx4 %1, %2, %3 offen lds" ::"s"(m0v), "v"(voff), "s"(rs), "s"(soff) : "memory");
}
DI void touch(unsigned m0v, unsigned voff, __amdgpu_buffer_rsrc_t rs, unsigned soff) {
    asm volatile("s_mov_b32 m0, %0\n\ts_nop 0\n\tbuffer_load_dword %1, %2, %3 offen lds" ::"s"(m0v), "v"(voff), "s"(rs), "s"(soff) : "memory");
}
template <int N> DI void wait_vm() { asm volatile("s_waitcnt vmcnt(%0)" ::"n"(N) : "memory"); }
DI void wait_lgkm0() { __builtin_amdgcn_s_waitcnt(0xC07F); }
DI void bar() { asm volatile("" ::: "memory"); __builtin_amdgcn_s_barrier(); asm volatile("" ::: "memory"); }
#define SCHED() __builtin_amdgcn_sched_barrier(0)
DI __amdgpu_buffer_rsrc_t mk_rsrc(const void* p, unsigned bytes) { return __builtin_amdgcn_make_buffer_rsrc(const_cast<void*>(p), 0, bytes, 0x00020000); }
DI bf16x8 lds_b128(LAS const char* p) { return *(LAS const bf16x8*)p; }
typedef short v4i16_t __attribute__((ext_vector_type(4)));
DI bf16x4 lds_tr(LAS const char* p) { return __builtin_amdgcn_ds_read_tr16_b64_v4i16((LAS v4i16_t*)p); }
DI bf16x8 cat(bf16x4 a, bf16x4 b) { return __builtin_shufflevector(a, b, 0, 1, 2, 3, 4, 5, 6, 7); }
DI f32x4 mfma(bf16x8 a, bf16x8 b, f32x4 c) { return __builtin_amdgcn_mfma_f32_16x16x32_bf16(a, b, c, 0, 0, 0); }
DI void st16(__amdgpu_buffer_rsrc_t rs, unsigned off, u32x4 v) { __builtin_amdgcn_raw_buffer_store_b128(v, rs, off, 0, 16); }
DI void st8(__amdgpu_buffer_rsrc_t rs, unsigned off, u32x2 v) { __builtin_amdgcn_raw_buffer_store_b64(v, rs, off, 0, 16); }
DI void st4(__amdgpu_buffer_rsrc_t rs, unsigned off, unsigned v) { __builtin_amdgcn_raw_buffer_store_b32(v, rs, off, 0, 16); }
DI u32x4 ld16(__amdgpu_buffer_rsrc_t rs, unsigned off) { return __builtin_amdgcn_raw_buffer_load_b128(rs, off, 0, 16); }
DI unsigned ld4(__amdgpu_buffer_rsrc_t rs, unsigned off) { return __builtin_amdgcn_raw_buffer_load_b32(rs, off, 0, 16); }
DI f32x2 row_stats(u32x4 v) {
    const float m0 = __uint_as_float(v[0]), q0 = __uint_as_float(v[1]), m1 = __uint_as_float(v[2]), q1 = __uint_as_float(v[3]);
    const float mean = sum8(m0 + m1) * (1.f / 16.f);
    const float d0 = m0 - mean, d1 = m1 - mean;
    const float q = sum8(q0 + q1 + 64.f * (d0 * d0 + d1 * d1));
    return (f32x2){mean, __builtin_amdgcn_rsqf(q * (1.f / 1024.f) + LN_EPS)};
}
DI unsigned blk_off(unsigned r, unsigned kc, unsigned KC) { return (((r >> 6) * KC + kc) << 10) + ((r & 63) << 4); }

template <int NT, int NC> DI int stage_of(int t, int r) {
    constexpr int N = NT - 1;
    const int cstar = r + NC * ((N - 1 - r) / NC);
    int c = t - 2;
    if (c >= cstar) c = t - 1;
    else if ((c & (NC - 1)) == r) c += NC;
    int ks = t == 0 ? 0 : (t == 1 ? r : c) + 1;
    asm volatile("" : "+s"(ks));
    return ks;
}
typedef std::integral_constant<bool, true> T_;
typedef std::integral_constant<bool, false> F_;

struct Ctx {
    LAS char* lds; char* ws; int tid, lane, w, b; u64 tag;
};
#define STAMP(c, i) do { } while (0)
#define STAMPC(c, i) do { } while (0)
#define STAMP_FLUSH(c, ph) do { } while (0)
constexpr int LDS_DUMMY = 140 * 1024;
constexpr int LDS_BYTES = LDS_DUMMY + 1024;

DI void fresh(Ctx& c) {
    int t = c.tid;
    asm volatile("" : "+v"(t), "+s"(c.b), "+s"(c.w), "+s"(c.ws));
    c.tid = t; c.lane = t & 63;
}
DI void signal(const Ctx& c, int set, int idx) {
    wait_vm<0>();
    bar();
    if (c.tid == 0) __hip_atomic_store((u64*)(c.ws + WS_FLAG) + set * 256 + idx, c.tag, __ATOMIC_RELAXED, __HIP_MEMORY_SCOPE_AGENT);
}
DI void wait_flags(const Ctx& c, int set, int idx0, int n) {
    const u64* f = (const u64*)(c.ws + WS_FLAG) + set * 256 + idx0 + (c.lane < n ? c.lane : 0);
    const u64 t0 = __builtin_amdgcn_s_memrealtime();
    while (!__all(__hip_atomic_load(f, __ATOMIC_RELAXED, __HIP_MEMORY_SCOPE_AGENT) == c.tag)) {
        __builtin_amdgcn_s_sleep(1);
        if (__builtin_amdgcn_s_memrealtime() - t0 > 2000000ull) break;
    }
}

DI void phase_ln(Ctx& c, const bf16_t* __restrict__ x, const bf16_t* __restrict__ wqkv) {
    const int r0 = c.b * 8, lane = c.lane, w = c.w;
    STAMP(c, 0);
    const u32x4* src = (const u32x4*)(x + (size_t)(r0 + w) * DM);
    const u32x4 a = src[lane], b = src[lane + 64];
    float v[16];
#pragma unroll
    for (int j = 0; j < 4; ++j) { v[2 * j] = bf_lo(a[j]); v[2 * j + 1] = bf_hi(a[j]); v[8 + 2 * j] = bf_lo(b[j]); v[8 + 2 * j + 1] = bf_hi(b[j]); }
    float s = 0.f;
#pragma unroll
    for (int j = 0; j < 16; ++j) s += v[j];
    float t = sum8(s); t += dpp<0x140>(t); t = xrow_sum(t);
    const float mean = t * (1.f / 1024.f);
    float q = 0.f;
#pragma unroll
    for (int j = 0; j < 16; ++j) { v[j] -= mean; q += v[j] * v[j]; }
    q = sum8(q); q += dpp<0x140>(q); q = xrow_sum(q);
    const float rs = __builtin_amdgcn_rsqf(q * (1.f / 1024.f) + LN_EPS);
    u32x4 oa, ob;
#pragma unroll
    for (int j = 0; j < 4; ++j) { oa[j] = cvt_pk(v[2 * j] * rs, v[2 * j + 1] * rs); ob[j] = cvt_pk(v[8 + 2 * j] * rs, v[8 + 2 * j + 1] * rs); }
    *(LAS u32x4*)(c.lds + (w * 129 + lane) * 16) = oa;
    *(LAS u32x4*)(c.lds + (w * 129 + lane + 64) * 16) = ob;
    wait_lgkm0();
    bar();
    const __amdgpu_buffer_rsrc_t ro = mk_rsrc(c.ws + WS_XN0, SEQ * DM * 2);
#pragma unroll
    for (int i = 0; i < 2; ++i) {
        const int idx = c.tid + 512 * i, kc = idx >> 3, rl = idx & 7;
        st16(ro, blk_off(r0 + rl, kc, 128), *(LAS const u32x4*)(c.lds + (rl * 129 + kc) * 16));
    }
    signal(c, F_XN, c.b);
    STAMP(c, 4); STAMP_FLUSH(c, 0);
    (void)wqkv;
}

template <int NSEG_, int NSUB_, int S_, int KSPAN_, int KFULL_, int PITCH_, int SEGSTEP_, int NC_>
struct SplitCfg {
    static constexpr int NSEG = NSEG_, NSUB = NSUB_, S = S_, KSPAN = KSPAN_, KFULL = KFULL_, PITCH = PITCH_, SEGSTEP = SEGSTEP_, NC = NC_;
    static constexpr int ABYTES = 2 * 4 * NSUB * 1024, BBYTES = NSUB * NSEG * 4096, STAGE = ABYTES + BBYTES;
    static constexpr int NT = KSPAN / (32 * NSUB);
    static constexpr int KC = KFULL / 8;
    static constexpr int PA = 2 * NSUB, PB = NSUB * NSEG, P = PA + PB;
    static constexpr int RING = S * STAGE;
};

template <class C>
DI void split_loader(Ctx& c, int lw, int cls, const char* A, unsigned a_bytes, unsigned a_base, const char* B, unsigned b_bytes, unsigned b_base) {
    const __amdgpu_buffer_rsrc_t ra = mk_rsrc(A, a_bytes), rb = mk_rsrc(B, b_bytes);
    const unsigned ldsb = (unsigned)(size_t)c.lds;
    const int lane = c.lane;
    const unsigned va = lane * 16;
    const unsigned rl = lane >> 3, sl = lane & 7;
    const unsigned vb = (8 * (rl >> 2) + (rl & 3)) * C::PITCH + (2 * ((sl >> 1) ^ ((rl >> 1) & 3)) + (sl & 1)) * 16;
    const unsigned a_src = a_base + (lw >> 1) * (C::KC * 1024) + (lw & 1) * (C::PA * 1024);
    const unsigned a_dst = (lw >> 1) * (4 * C::NSUB * 1024) + (lw & 1) * (C::PA * 1024);
    const unsigned b_src = b_base + (16 * (lw >> 1) + 4 * (lw & 1)) * C::PITCH;
    const unsigned b_dst = C::ABYTES + lw * 1024;
    auto issueA = [&](int tt) {
        const unsigned t = stage_of<C::NT, C::NC>(tt, cls);
        const unsigned sb = ldsb + (unsigned)(tt % C::S) * C::STAGE;
#pragma unroll
        for (int p = 0; p < C::PA; ++p) dma16<true>(sb + a_dst + p * 1024, va, ra, a_src + (t * 4 * C::NSUB + p) * 1024);
    };
    auto issueB = [&](int tt) {
        const unsigned t = stage_of<C::NT, C::NC>(tt, cls);
        const unsigned sb = ldsb + (unsigned)(tt % C::S) * C::STAGE;
#pragma unroll
        for (int u = 0; u < C::NSUB; ++u)
#pragma unroll
            for (int g = 0; g < C::NSEG; ++g) dma16<false>(sb + b_dst + (u * C::NSEG + g) * 4096, vb, rb, b_src + (t * C::NSUB + u) * 32 * C::PITCH + g * C::SEGSTEP);
    };
    auto issue = [&](int tt) { issueA(tt); issueB(tt); };
    static_assert(C::S == 3 || C::S == 4, "ring depth");
    issueB(0); issueB(1);
    bar();
    issueA(0); issueA(1);
    wait_vm<C::PA>();
    bar();
    issue(2);
    wait_vm<C::P>();
    bar();
    int nx = 3;
    if (C::S == 4) { issue(3); nx = 4; }
    issue(nx); ++nx;
    for (; nx < C::NT; ++nx) {
        wait_vm<C::P*(C::S - 2)>();
        bar();
        issue(nx);
    }
    wait_vm<C::P*(C::S - 2)>(); bar();
    if (C::S == 4) { wait_vm<C::P>(); bar(); }
    wait_vm<0>(); bar();
}

template <class C> struct Frags { bf16x8 X[4]; bf16x4 W[C::NSEG][2][2]; };

template <class C, int TMASK>
struct Consumer {
    LAS char* lds; unsigned xa, wa[2];
    DI Consumer(LAS char* lds_, int cw, int lane) : lds(lds_) {
        const int wm = cw & 1, wn = cw >> 1;
        const int g = lane >> 4, fr = lane & 15, q = (lane & 15) >> 2, p = lane & 3;
        xa = wm * (4 * C::NSUB * 1024) + g * 1024 + fr * 16;
        const unsigned rho = 16 * (g >> 1) + 4 * (g & 1) + q, xr = 2 * (g & 1) + (q >> 1);
#pragma unroll
        for (int dt = 0; dt < 2; ++dt) wa[dt] = C::ABYTES + rho * 128 + (((2 * wn + dt) ^ xr) << 5) + (p >> 1) * 16 + (p & 1) * 8;
    }
    static constexpr int NR = 4 + 4 * C::NSEG;
    static constexpr int NM = 8 * C::NSEG;
    DI void load1(Frags<C>& f, unsigned sb, int u, int i) const {
        if (i >= 2 && i < 6) f.X[i - 2] = lds_b128(lds + sb + xa + u * 4096 + (i - 2) * 256);
        else {
            const int k = i < 2 ? i : i - 4, s = k >> 2, dt = (k >> 1) & 1, h = k & 1;
            f.W[s][dt][h] = lds_tr(lds + sb + wa[dt] + (u * C::NSEG + s) * 4096 + h * 1024);
        }
    }
    DI static bf16x8 wfrag(const Frags<C>& f, int s, int dt) { return cat(f.W[s][dt][0], f.W[s][dt][1]); }
    DI void mma1(const Frags<C>& f, f32x4 (&acc)[C::NSEG][2][4], int i) const {
        const int s = i >> 3, dt = (i >> 2) & 1, mt = i & 3;
        if ((TMASK >> s) & 1) acc[s][dt][mt] = mfma(wfrag(f, s, dt), f.X[mt], acc[s][dt][mt]);
        else acc[s][dt][mt] = mfma(f.X[mt], wfrag(f, s, dt), acc[s][dt][mt]);
    }
};

template <class C, int TMASK, class Extra>
DI void split_consumer(Ctx& c, f32x4 (&acc)[C::NSEG][2][4], Extra extra) {
    typedef Consumer<C, TMASK> K;
    const K k(c.lds, c.w, c.lane);
    auto sub = [&](const Frags<C>& cur, Frags<C>& nxt, unsigned sb, int u, auto READ) {
#pragma unroll
        for (int i = 0; i < K::NM; ++i) {
            k.mma1(cur, acc, i);
            if constexpr (decltype(READ)::value) { if (i < K::NR) k.load1(nxt, sb, u, i); }
            SCHED();
        }
    };
    Frags<C> f0, f1;
    bar();
    STAMP(c, 2);
#pragma unroll
    for (int i = 0; i < K::NR; ++i) k.load1(f0, 0, 0, i);
    wait_lgkm0();
    unsigned slot = 0;
    static_assert(C::NSUB == 2, "NSUB");
    for (int t = 0; t < C::NT - 1; ++t) {
        const unsigned sb = slot * C::STAGE;
        slot = (slot + 1 == C::S) ? 0 : slot + 1;
        sub(f0, f1, sb, 1, T_{});
        extra(f0, t);
        wait_lgkm0();
        bar();
        sub(f1, f0, slot * C::STAGE, 0, T_{});
        extra(f1, t);
        wait_lgkm0();
    }
    sub(f0, f1, slot * C::STAGE, 1, T_{});
    extra(f0, C::NT - 1);
    wait_lgkm0();
    sub(f1, f0, 0, 0, F_{});
    extra(f1, C::NT - 1);
    STAMP(c, 3);
}

template <class C, int TMASK>
DI void split_consumer_stage(Ctx& c, f32x4 (&acc)[C::NSEG][2][4]) {
    typedef Consumer<C, TMASK> K;
    const K k(c.lds, c.w, c.lane);
    struct SF { Frags<C> u[C::NSUB]; };
    constexpr int NM = K::NM * C::NSUB, NR = K::NR * C::NSUB;
    auto step = [&](const SF& cur, SF& nxt, unsigned sb, auto READ, auto BARR) {
#pragma unroll
        for (int i = 0; i < NM; ++i) {
            k.mma1(cur.u[i / K::NM], acc, i % K::NM);
            if constexpr (decltype(READ)::value) {
                if constexpr (NR <= NM * 3 / 4) {
                    if (i < NR) k.load1(nxt.u[i / K::NR], sb, i / K::NR, i % K::NR);
                } else {
                    if (2 * i < NR) k.load1(nxt.u[(2 * i) / K::NR], sb, (2 * i) / K::NR, (2 * i) % K::NR);
                    if (2 * i + 1 < NR) k.load1(nxt.u[(2 * i + 1) / K::NR], sb, (2 * i + 1) / K::NR, (2 * i + 1) % K::NR);
                }
            }
            SCHED();
        }
        wait_lgkm0();
        if constexpr (decltype(BARR)::value) bar();
    };
    SF f0, f1;
    bar();
    STAMP(c, 2);
#pragma unroll
    for (int i = 0; i < NR; ++i) k.load1(f0.u[i / K::NR], 0, i / K::NR, i % K::NR);
    wait_lgkm0();
    bar();
    static_assert(C::NT % 2 == 0 && C::S == 4, "stage loop");
    for (int t = 0; t < C::NT - 2; t += 2) {
        step(f0, f1, ((t + 1) & 3) * C::STAGE, T_{}, T_{});
        step(f1, f0, ((t + 2) & 3) * C::STAGE, T_{}, T_{});
    }
    step(f0, f1, ((C::NT - 1) & 3) * C::STAGE, T_{}, F_{});
    step(f1, f0, 0, F_{}, F_{});
    STAMP(c, 3);
}

typedef SplitCfg<3, 2, 3, DM, DM, 3 * DM * 2, 2048, 4> CfgQKV;

template <int L>
DI void phase_qkv(Ctx& c, const bf16_t* __restrict__ wqkv) {
    typedef CfgQKV C;
    constexpr bool LNFUSE = L == 1;
    LAS char* lds = c.lds; char* ws = c.ws;
    const int lane = c.lane, w = c.w;
    const int xcd = c.b & 7, j = c.b >> 3;
    const int pm = 8 * (xcd & 1) + (j & 7), hp = 4 * (xcd >> 1) + (j >> 3);
    const int cls = (j + (j >> 3)) & 3;
    STAMP(c, 0);
    if (w >= 4) {
        split_loader<C>(c, w - 4, cls, ws + (L == 0 ? WS_XN0 : WS_X2), SEQ * DM * 2, pm * 2 * (C::KC * 1024), (const char*)wqkv, DM * 3 * DM * 2, hp * 128);
        if (LNFUSE) bar();
        bar();
        bar();
        signal(c, F_QKV + F_PER_LAYER * L, hp * 16 + pm);
        return;
    }
    if (w == 0) wait_flags(c, L == 0 ? F_XN : F_DN, pm * 16, 16);
    bar();
    STAMP(c, 1);
    const int wm = w & 1, hh = w >> 1, g = lane >> 4, fr = lane & 15;
    u32x4 sv[4], cv[3];
    if (LNFUSE) {
        const __amdgpu_buffer_rsrc_t rs = mk_rsrc(ws + WS_ST2, SEQ * 128), rc = mk_rsrc(ws + WS_CSQ, 3 * DM * 64);
#pragma unroll
        for (int i = 0; i < 4; ++i) sv[i] = ld16(rs, (pm * 128 + w * 32 + 8 * i + (lane >> 3)) * 128 + (lane & 7) * 16);
#pragma unroll
        for (int i = 0; i < 3; ++i) {
            const int cc = 48 * w + 16 * i + (lane >> 2);
            cv[i] = ld16(rc, ((cc >> 6) * DM + hp * 64 + (cc & 63)) * 64 + (lane & 3) * 16);
        }
    }
    f32x4 acc[3][2][4];
#pragma unroll
    for (int s = 0; s < 3; ++s)
#pragma unroll
        for (int d = 0; d < 2; ++d)
#pragma unroll
            for (int m = 0; m < 4; ++m) acc[s][d][m] = (f32x4){0.f, 0.f, 0.f, 0.f};
    const bf16x8 ones = {0x3F80, 0x3F80, 0x3F80, 0x3F80, 0x3F80, 0x3F80, 0x3F80, 0x3F80};
    f32x4 csum[3] = {{0.f, 0.f, 0.f, 0.f}, {0.f, 0.f, 0.f, 0.f}, {0.f, 0.f, 0.f, 0.f}};
    auto extra = [&](const Frags<C>& f, int t) {
        if constexpr (!LNFUSE) {
            if (stage_of<C::NT, C::NC>(t, cls) == pm) {
                typedef Consumer<C, 1> K;
                if (wm == 0) { csum[0] = mfma(K::wfrag(f, 0, 0), ones, csum[0]); csum[1] = mfma(K::wfrag(f, 0, 1), ones, csum[1]); csum[2] = mfma(K::wfrag(f, 1, 0), ones, csum[2]); }
                else { csum[0] = mfma(K::wfrag(f, 1, 1), ones, csum[0]); csum[1] = mfma(K::wfrag(f, 2, 0), ones, csum[1]); csum[2] = mfma(K::wfrag(f, 2, 1), ones, csum[2]); }
            }
        }
    };
    split_consumer<C, 1>(c, acc, extra);

    LAS f32x2* st = (LAS f32x2*)(lds + C::RING);
    LAS float* cs = (LAS float*)(lds + C::RING + 1024);
    if (LNFUSE) {
#pragma unroll
        for (int i = 0; i < 4; ++i) { const f32x2 r = row_stats(sv[i]); if ((lane & 7) == 0) st[w * 32 + 8 * i + (lane >> 3)] = r; }
#pragma unroll
        for (int i = 0; i < 3; ++i) {
            const float cc = sum4((__uint_as_float(cv[i][0]) + __uint_as_float(cv[i][1])) + (__uint_as_float(cv[i][2]) + __uint_as_float(cv[i][3])));
            if ((lane & 3) == 0) cs[48 * w + 16 * i + (lane >> 2)] = cc;
        }
        wait_lgkm0();
        bar();
    } else if (fr == 0) {
        const __amdgpu_buffer_rsrc_t rc = mk_rsrc(ws + WS_CSQ, 3 * DM * 64);
#pragma unroll
        for (int k = 0; k < 3; ++k) {
            const int f = 3 * wm + k, s = f >> 1, dt = f & 1;
#pragma unroll
            for (int i = 0; i < 4; ++i) st4(rc, (s * DM + hp * 64 + hh * 32 + 16 * dt + 4 * g + i) * 64 + pm * 4, __float_as_uint(csum[k][i]));
        }
    }
    const int head = hp * 2 + hh;
    bf16x8 kf[2][2], vf[2][2];
    {
        f32x2 nm[4][2], rs[4][2];
        if (LNFUSE) {
#pragma unroll
            for (int mt = 0; mt < 4; ++mt)
#pragma unroll
                for (int i = 0; i < 2; ++i) {
                    const f32x4 s = *(LAS const f32x4*)(st + wm * 64 + 16 * mt + 4 * g + 2 * i);
                    nm[mt][i] = (f32x2){-s[0], -s[2]}; rs[mt][i] = (f32x2){s[1], s[3]};
                }
        }
#pragma unroll
        for (int dt = 0; dt < 2; ++dt) {
            float ck = 0.f, cvv = 0.f;
            if (LNFUSE) { ck = cs[64 + hh * 32 + 16 * dt + fr]; cvv = cs[128 + hh * 32 + 16 * dt + fr]; }
#pragma unroll
            for (int sp = 0; sp < 2; ++sp) {
                u32x4 ku, vu;
#pragma unroll
                for (int h2 = 0; h2 < 2; ++h2) {
                    const int mt = 2 * sp + h2;
#pragma unroll
                    for (int i = 0; i < 2; ++i) {
                        f32x2 tk = {acc[1][dt][mt][2 * i], acc[1][dt][mt][2 * i + 1]}, tv = {acc[2][dt][mt][2 * i], acc[2][dt][mt][2 * i + 1]};
                        if (LNFUSE) { tk = (tk + nm[mt][i] * ck) * rs[mt][i]; tv = (tv + nm[mt][i] * cvv) * rs[mt][i]; }
                        ku[2 * h2 + i] = cvt_pk2(phi2(tk)); vu[2 * h2 + i] = cvt_pk2(tv);
                    }
                }
                kf[dt][sp] = __builtin_bit_cast(bf16x8, ku); vf[dt][sp] = __builtin_bit_cast(bf16x8, vu);
            }
        }
    }
    f32x4 kv[2][2], z[2];
#pragma unroll
    for (int dt = 0; dt < 2; ++dt) {
        z[dt] = mfma(kf[dt][0], ones, (f32x4){0.f, 0.f, 0.f, 0.f});
        z[dt] = mfma(kf[dt][1], ones, z[dt]);
#pragma unroll
        for (int et = 0; et < 2; ++et) {
            kv[dt][et] = mfma(kf[dt][0], vf[et][0], (f32x4){0.f, 0.f, 0.f, 0.f});
            kv[dt][et] = mfma(kf[dt][1], vf[et][1], kv[dt][et]);
        }
    }
    const __amdgpu_buffer_rsrc_t rq = mk_rsrc(ws + WS_PQ + L * 4 * MB, SEQ * DM * 2);
#pragma unroll
    for (int mt = 0; mt < 4; ++mt) {
        float nmean = 0.f, rstd = 1.f;
        if (LNFUSE) { const f32x2 s = st[wm * 64 + 16 * mt + fr]; nmean = -s.x; rstd = s.y; }
        unsigned pk[2][2];
#pragma unroll
        for (int dt = 0; dt < 2; ++dt)
#pragma unroll
            for (int i = 0; i < 2; ++i) {
                f32x2 t = {acc[0][dt][mt][2 * i], acc[0][dt][mt][2 * i + 1]};
                if (LNFUSE) t = (t + *(LAS const f32x2*)(cs + hh * 32 + 16 * dt + 4 * g + 2 * i) * nmean) * rstd;
                pk[dt][i] = cvt_pk2(phi2(t));
            }
        swap16(pk[0][0], pk[1][0]); swap16(pk[0][1], pk[1][1]);
        const int ch = ((g & 1) << 1) | (g >> 1);
        st16(rq, blk_off(pm * 128 + wm * 64 + 16 * mt + fr, head * 4 + ch, 128), (u32x4){pk[0][0], pk[0][1], pk[1][0], pk[1][1]});
    }
    bar();
    LAS f32x4* red = (LAS f32x4*)(lds + hh * 8192);
    if (wm == 1) {
#pragma unroll
        for (int dt = 0; dt < 2; ++dt) {
#pragma unroll
            for (int et = 0; et < 2; ++et) red[(dt * 2 + et) * 64 + lane] = kv[dt][et];
            red[(4 + dt) * 64 + lane] = z[dt];
        }
        wait_lgkm0();
    }
    bar();
    if (wm == 0) {
        const __amdgpu_buffer_rsrc_t rk = mk_rsrc(ws + WS_KVP + L * 4 * MB, 16 * NH * KVP_STRIDE * 4);
        const unsigned dst = (pm * NH + head) * KVP_STRIDE * 4;
#pragma unroll
        for (int dt = 0; dt < 2; ++dt) {
#pragma unroll
            for (int et = 0; et < 2; ++et) st16(rk, dst + ((dt * 2 + et) * 64 + lane) * 16, __builtin_bit_cast(u32x4, kv[dt][et] + red[(dt * 2 + et) * 64 + lane]));
            const f32x4 zz = z[dt] + red[(4 + dt) * 64 + lane];
            if (fr == 0) st16(rk, dst + (1024 + 16 * dt + 4 * g) * 4, __builtin_bit_cast(u32x4, zz));
        }
    }
    signal(c, F_QKV + F_PER_LAYER * L, hp * 16 + pm);
    STAMP(c, 4); STAMP_FLUSH(c, 1 + 5 * L);
}

template <int L>
DI void phase_attn(Ctx& c) {
    LAS char* sm = c.lds;
    char* ws = c.ws;
    const int tid = c.tid, lane = c.lane, w = c.w, g = lane >> 4, fr = lane & 15;
    const int xcd = c.b & 7, j = c.b >> 3;
    const int head = 4 * xcd + (j & 3), pb = j >> 2;
    STAMP(c, 0);
    if (w == 0) wait_flags(c, F_QKV + F_PER_LAYER * L, (head >> 1) * 16, 16);
    bar();
    STAMP(c, 1);
    const __amdgpu_buffer_rsrc_t rk = mk_rsrc(ws + WS_KVP + L * 4 * MB, 16 * NH * KVP_STRIDE * 4), rq = mk_rsrc(ws + WS_PQ + L * 4 * MB, SEQ * DM * 2);
    bf16x8 qf[2];
#pragma unroll
    for (int mt = 0; mt < 2; ++mt) qf[mt] = __builtin_bit_cast(bf16x8, ld16(rq, blk_off(pb * 256 + w * 32 + 16 * mt + fr, head * 4 + g, 128)));
    const int half = w >> 2, t4 = tid & 255;
    f32x4 s = {0.f, 0.f, 0.f, 0.f}, zs = {0.f, 0.f, 0.f, 0.f};
    {
        f32x4 p[8], pz[8];
#pragma unroll
        for (int i = 0; i < 8; ++i) p[i] = __builtin_bit_cast(f32x4, ld16(rk, ((half * 8 + i) * NH + head) * (KVP_STRIDE * 4) + t4 * 16));
        if (t4 < 8) {
#pragma unroll
            for (int i = 0; i < 8; ++i) pz[i] = __builtin_bit_cast(f32x4, ld16(rk, ((half * 8 + i) * NH + head) * (KVP_STRIDE * 4) + 4096 + t4 * 16));
#pragma unroll
            for (int i = 0; i < 8; ++i) zs += pz[i];
        }
#pragma unroll
        for (int i = 0; i < 8; ++i) s += p[i];
    }
    if (half == 1) { *(LAS f32x4*)(sm + 4096 + t4 * 16) = s; if (t4 < 8) *(LAS f32x4*)(sm + 8192 + t4 * 16) = zs; }
    wait_lgkm0();
    bar();
    if (half == 0) {
        s += *(LAS const f32x4*)(sm + 4096 + t4 * 16);
        const int dt = w >> 1, et = w & 1;
        *(LAS u32x2*)(sm + (16 * et + fr) * 80 + (16 * dt + 4 * g) * 2) = (u32x2){cvt_pk(s[0], s[1]), cvt_pk(s[2], s[3])};
        if (t4 < 8) { zs += *(LAS const f32x4*)(sm + 8192 + t4 * 16); *(LAS u32x2*)(sm + 2560 + t4 * 8) = (u32x2){cvt_pk(zs[0], zs[1]), cvt_pk(zs[2], zs[3])}; }
    }
    wait_lgkm0();
    bar();
    bf16x8 kf[2];
#pragma unroll
    for (int et = 0; et < 2; ++et) kf[et] = lds_b128(sm + (16 * et + fr) * 80 + g * 16);
    const bf16x8 zf = lds_b128(sm + 2560 + g * 16);
    const f32x4 zero = {0.f, 0.f, 0.f, 0.f};
    const __amdgpu_buffer_rsrc_t ro = mk_rsrc(ws + WS_O + L * 4 * MB, SEQ * DM * 2);
#pragma unroll
    for (int mt = 0; mt < 2; ++mt) {
        const f32x4 den = mfma(zf, qf[mt], zero);
        const f32x4 n0 = mfma(kf[0], qf[mt], zero), n1 = mfma(kf[1], qf[mt], zero);
        const float r = __builtin_amdgcn_rcpf(den[0] + 1e-6f);
        unsigned a0 = cvt_pk(n0[0] * r, n0[1] * r), a1 = cvt_pk(n0[2] * r, n0[3] * r);
        unsigned b0 = cvt_pk(n1[0] * r, n1[1] * r), b1 = cvt_pk(n1[2] * r, n1[3] * r);
        swap16(a0, b0); swap16(a1, b1);
        const int ch = ((g & 1) << 1) | (g >> 1);
        st16(ro, blk_off(pb * 256 + w * 32 + 16 * mt + fr, head * 4 + ch, 128), (u32x4){a0, a1, b0, b1});
    }
    signal(c, F_ATT + F_PER_LAYER * L, pb * 32 + head);
    STAMP(c, 4); STAMP_FLUSH(c, 2 + 5 * L);
}

template <bool OUT_RM>
DI void epi_n1024(Ctx& c, f32x4 (&acc)[2][4], const u32x4 (&rv)[4], int pm, int pn, char* out, char* stats) {
    const int lane = c.lane, w = c.w, wm = w & 1, wn = w >> 1, g = lane >> 4, fr = lane & 15;
    const int kc = pn * 8 + wn * 4 + (((g & 1) << 1) | (g >> 1));
    const __amdgpu_buffer_rsrc_t ro = mk_rsrc(out, SEQ * DM * 2);
    f32x2 part[4];
#pragma unroll
    for (int mt = 0; mt < 4; ++mt) {
        float v[8];
#pragma unroll
        for (int i = 0; i < 4; ++i) { float a = acc[0][mt][i], b = acc[1][mt][i]; swap16f(a, b); v[i] = a; v[4 + i] = b; }
        u32x4 o;
#pragma unroll
        for (int i = 0; i < 4; ++i) o[i] = cvt_pk(v[2 * i] + bf_lo(rv[mt][i]), v[2 * i + 1] + bf_hi(rv[mt][i]));
        const int row = pm * 128 + wm * 64 + 16 * mt + fr;
        if (OUT_RM) st16(ro, row * (DM * 2) + kc * 16, o);
        else {
            st16(ro, blk_off(row, kc, 128), o);
            float s = 0.f;
#pragma unroll
            for (int i = 0; i < 4; ++i) { v[2 * i] = bf_lo(o[i]); v[2 * i + 1] = bf_hi(o[i]); s += v[2 * i] + v[2 * i + 1]; }
            const float m = xrow_sum(s) * (1.f / 32.f);
            float q = 0.f;
#pragma unroll
            for (int i = 0; i < 8; ++i) q += (v[i] - m) * (v[i] - m);
            part[mt] = (f32x2){m, xrow_sum(q)};
        }
    }
    if (!OUT_RM) {
        bar();
        LAS f32x2* sp = (LAS f32x2*)c.lds;
        if (wn == 1 && g == 0) {
#pragma unroll
            for (int mt = 0; mt < 4; ++mt) sp[wm * 64 + 16 * mt + fr] = part[mt];
        }
        wait_lgkm0();
        bar();
        if (wn == 0 && g == 0) {
#pragma unroll
            for (int mt = 0; mt < 4; ++mt) {
                const f32x2 o = sp[wm * 64 + 16 * mt + fr];
                const float d = part[mt].x - o.x;
                const f32x2 r = {0.5f * (part[mt].x + o.x), part[mt].y + o.y + 16.f * d * d};
                st8(mk_rsrc(stats, SEQ * 128), (pm * 128 + wm * 64 + 16 * mt + fr) * 128 + pn * 8, __builtin_bit_cast(u32x2, r));
            }
        }
    }
}

typedef SplitCfg<1, 2, 4, DM, DM, DM * 2, 0, 4> CfgWO;
template <int L>
DI void phase_wo(Ctx& c, const bf16_t* __restrict__ wo, const bf16_t* __restrict__ x) {
    typedef CfgWO C;
    char* ws = c.ws;
    const int lane = c.lane, w = c.w;
    const int xcd = c.b & 7, j = c.b >> 3;
    const int pm = 4 * (xcd & 3) + (j & 3), pn = 8 * (xcd >> 2) + (j >> 2);
    const int fidx = (pm >> 1) * 32 + (pm & 1) * 16 + pn;
    STAMP(c, 0);
    if (w >= 4) {
        split_loader<C>(c, w - 4, (j + (j >> 2)) & 3, ws + WS_O + L * 4 * MB, SEQ * DM * 2, pm * 2 * (C::KC * 1024), (const char*)wo, DM * DM * 2, pn * 128);
        bar(); bar();
        signal(c, F_WO + F_PER_LAYER * L, fidx);
        return;
    }
    if (w == 0) wait_flags(c, F_ATT + F_PER_LAYER * L, (pm >> 1) * 32, 32);
    bar();
    STAMP(c, 1);
    const int wm = w & 1, wn = w >> 1, g = lane >> 4, fr = lane & 15;
    const int kc = pn * 8 + wn * 4 + (((g & 1) << 1) | (g >> 1));
    u32x4 rv[4];
#pragma unroll
    for (int mt = 0; mt < 4; ++mt) {
        const int row = pm * 128 + wm * 64 + 16 * mt + fr;
        rv[mt] = L == 0 ? *(const u32x4*)((const char*)x + (size_t)row * (DM * 2) + kc * 16) : ld16(mk_rsrc(ws + WS_X2, SEQ * DM * 2), blk_off(row, kc, 128));
    }
    f32x4 acc[1][2][4];
#pragma unroll
    for (int d = 0; d < 2; ++d)
#pragma unroll
        for (int m = 0; m < 4; ++m) acc[0][d][m] = (f32x4){0.f, 0.f, 0.f, 0.f};
    split_consumer_stage<C, 1>(c, acc);
    epi_n1024<false>(c, acc[0], rv, pm, pn, ws + WS_X1 + L * 4 * MB, ws + WS_ST1 + L * MB);
    signal(c, F_WO + F_PER_LAYER * L, fidx);
    STAMP(c, 4); STAMP_FLUSH(c, 3 + 5 * L);
}

typedef SplitCfg<1, 2, 4, FF, FF, DM * 2, 0, 4> CfgDN;
template <int L>
DI void phase_down(Ctx& c, const bf16_t* __restrict__ wd, char* __restrict__ out) {
    typedef CfgDN C;
    constexpr bool OUT_RM = L == 1;
    char* ws = c.ws;
    const int lane = c.lane, w = c.w;
    const int xcd = c.b & 7, j = c.b >> 3;
    const int pm = 4 * (xcd & 3) + (j & 3), pn = 8 * (xcd >> 2) + (j >> 2);
    STAMP(c, 0);
    if (w >= 4) {
        split_loader<C>(c, w - 4, (j + (j >> 2)) & 3, ws + WS_H + L * 16 * MB, SEQ * FF * 2, pm * 2 * (C::KC * 1024), (const char*)wd, FF * DM * 2, pn * 128);
        if (!OUT_RM) { bar(); bar(); signal(c, F_DN, pm * 16 + pn); }
        return;
    }
    if (w == 0) wait_flags(c, F_GU + F_PER_LAYER * L, (pm >> 1) * 32, 32);
    bar();
    STAMP(c, 1);
    const int wm = w & 1, wn = w >> 1, g = lane >> 4, fr = lane & 15;
    const int kc = pn * 8 + wn * 4 + (((g & 1) << 1) | (g >> 1));
    u32x4 rv[4];
#pragma unroll
    for (int mt = 0; mt < 4; ++mt) rv[mt] = ld16(mk_rsrc(ws + WS_X1 + L * 4 * MB, SEQ * DM * 2), blk_off(pm * 128 + wm * 64 + 16 * mt + fr, kc, 128));
    f32x4 acc[1][2][4];
#pragma unroll
    for (int d = 0; d < 2; ++d)
#pragma unroll
        for (int m = 0; m < 4; ++m) acc[0][d][m] = (f32x4){0.f, 0.f, 0.f, 0.f};
    split_consumer_stage<C, 1>(c, acc);
    epi_n1024<OUT_RM>(c, acc[0], rv, pm, pn, OUT_RM ? out : ws + WS_X2, ws + WS_ST2);
    if (!OUT_RM) signal(c, F_DN, pm * 16 + pn);
    STAMP(c, 4); STAMP_FLUSH(c, 5 + 5 * L);
}

#define GU_LEAD 3
#define GU_NC 4
constexpr int GU_S = 4;
constexpr int GU_GOFF = 16384, GU_UOFF = 16384 + 7168 + 16, GU_STAGE = GU_UOFF + 7168;
constexpr int GU_RING = GU_S * GU_STAGE;
constexpr int GU_STATS = GU_RING, GU_CS = GU_STATS + 2048;
static_assert(GU_CS + 1024 <= LDS_DUMMY && CfgDN::RING <= LDS_DUMMY && CfgQKV::RING + 4096 <= LDS_DUMMY, "LDS map");
struct GuFrags { bf16x8 X[4]; bf16x4 W[7][2]; };

template <int L>
DI void phase_gateup(Ctx& c, const bf16_t* __restrict__ wg, const bf16_t* __restrict__ wu) {
    constexpr bool FIRST = L == 0;
    LAS char* lds = c.lds; char* ws = c.ws;
    const int tid = c.tid, lane = c.lane, w = c.w;
    const int xcd = c.b & 7, jj = c.b >> 3;
    const int pm = 4 * (xcd & 1) + (jj & 3), pn = 8 * (xcd >> 1) + (jj >> 2);
    const int wr = w >> 1, wc = w & 1, g = lane >> 4, fr = lane & 15, q = fr >> 2, p = lane & 3;
    STAMP(c, 0);

    const __amdgpu_buffer_rsrc_t ra = mk_rsrc(ws + WS_X1 + L * 4 * MB, SEQ * DM * 2);
    const __amdgpu_buffer_rsrc_t rg = mk_rsrc(wg, w <= 6 ? DM * FF * 2 : 0), ru = mk_rsrc(wu, w >= 1 ? DM * FF * 2 : 0);
    const unsigned ldsb = (unsigned)(size_t)lds;
    const unsigned va = lane * 16;
    auto bpat = [&](int piece) { const unsigned i = 64 * piece + lane, rho = i / 14, cc = i % 14; const unsigned k = (rho & 0x13) | ((rho & 8) >> 1) | ((rho & 4) << 1); return k * (FF * 2) + cc * 16; };
    const unsigned vg = bpat(w <= 6 ? w : 0), vu = bpat(w >= 1 ? w - 1 : 0);
    const unsigned a_src = (pm * 4 + (w >> 1)) * (128 * 1024) + (w & 1) * 2048, a_dst = (w >> 1) * 4096 + (w & 1) * 2048;
    const unsigned g_dst0 = w <= 6 ? GU_GOFF + w * 1024 : LDS_DUMMY, g_str = w <= 6 ? GU_STAGE : 0;
    const unsigned u_dst0 = w >= 1 ? GU_UOFF + (w - 1) * 1024 : LDS_DUMMY, u_str = w >= 1 ? GU_STAGE : 0;
    const unsigned b_src = pn * 224;
    const int cls = ((jj & 3) + (jj >> 2)) & (GU_NC - 1);
    auto issue1 = [&](int ks, unsigned slot, int i) {
        if (i < 2) dma16<true>(ldsb + slot * GU_STAGE + a_dst + i * 1024, va, ra, a_src + (ks * 4 + i) * 1024);
        else if (i == 2) dma16<false>(ldsb + g_dst0 + slot * g_str, vg, rg, b_src + ks * (32 * FF * 2));
        else dma16<false>(ldsb + u_dst0 + slot * u_str, vu, ru, b_src + ks * (32 * FF * 2));
    };
    auto issue = [&](int t, int i0, int i1) {
        const int ks = stage_of<32, GU_NC>(t, cls);
#pragma unroll
        for (int i = 0; i < 4; ++i) if (i >= i0 && i < i1) issue1(ks, t & 3, i);
    };
    issue(0, 2, 4); issue(1, 2, 4);
    if (w == 0) wait_flags(c, F_WO + F_PER_LAYER * L, pm * 32, 32);
    bar();
    STAMP(c, 1);
    issue(0, 0, 2); issue(1, 0, 2);
    u32x4 sv[4]; float cv = 0.f;
    {
        const __amdgpu_buffer_rsrc_t rs = mk_rsrc(ws + WS_ST1 + L * MB, SEQ * 128), rc = mk_rsrc(ws + WS_CSG, 2 * FF * 4);
        const unsigned so = (pm * 256 + w * 32 + (lane >> 3)) * 128 + (lane & 7) * 16;
#pragma unroll
        for (int i = 0; i < 4; ++i) asm volatile("buffer_load_dwordx4 %0, %1, %2, 0 offen sc1" : "=v"(sv[i]) : "v"(so + i * 1024), "s"(rs) : "memory");
        const int cc = tid < 224 ? tid : 0;
        const unsigned co = ((cc >= 112 ? FF + cc - 112 : cc) + pn * 112) * 4;
        if (!FIRST) asm volatile("buffer_load_dword %0, %1, %2, 0 offen sc1" : "=v"(cv) : "v"(co), "s"(rc) : "memory");
    }
    constexpr int NX = FIRST ? 4 : 5;

    const unsigned xa = wr * 4096 + g * 1024 + fr * 16;
    const unsigned rho0 = 16 * (g >> 1) + 4 * (g & 1) + q;
    const unsigned wa = (rho0 * 14 + 7 * wc + (p >> 1)) * 16 + (p & 1) * 8;
    const unsigned wm_ = (rho0 * 14 + 7 * wc + 6) * 16 + (p & 1) * 8 + ((p >> 1) ? GU_UOFF : GU_GOFF);
    auto load1 = [&](GuFrags& f, unsigned sb, int i) {
        if (i >= 2 && i < 6) f.X[i - 2] = lds_b128(lds + sb + xa + (i - 2) * 256);
        else {
            const int k = i < 2 ? i : i - 4, T = k >> 1, h = k & 1;
            LAS const char* pw = T < 3 ? lds + sb + wa + GU_GOFF + T * 32 : (T < 6 ? lds + sb + wa + GU_UOFF + (T - 3) * 32 : lds + sb + wm_);
            f.W[T][h] = lds_tr(pw + h * 1792);
        }
    };

    wait_vm<2 + NX>();
    bar();
    STAMP(c, 2);
    GuFrags f0, f1;
#pragma unroll
    for (int i = 0; i < 18; ++i) load1(f0, 0, i);
    issue(2, 0, 4);
    if (GU_LEAD == 4) issue(3, 0, 4);
    wait_vm<NX + 4 * (GU_LEAD - 2)>();
    wait_lgkm0();
    bar();

    f32x4 acc[7][4];
#pragma unroll
    for (int T = 0; T < 7; ++T)
#pragma unroll
        for (int m = 0; m < 4; ++m) acc[T][m] = (f32x4){0.f, 0.f, 0.f, 0.f};
    f32x4 csa = {0.f, 0.f, 0.f, 0.f}, csb = {0.f, 0.f, 0.f, 0.f};
    const bf16x8 ones = {0x3F80, 0x3F80, 0x3F80, 0x3F80, 0x3F80, 0x3F80, 0x3F80, 0x3F80};

    auto step = [&](const GuFrags& cur, GuFrags& nxt, int t, auto DMA, auto READ, auto VM) {
        const unsigned nsb = ((t + 1) & (GU_S - 1)) * GU_STAGE;
        const unsigned dslot = (t + GU_LEAD) & (GU_S - 1);
        const int ks = stage_of<32, GU_NC>(t + GU_LEAD, cls);
#pragma unroll
        for (int i = 0; i < 28; ++i) {
            const int T = i >> 2, m = i & 3;
            acc[T][m] = mfma(cat(cur.W[T][0], cur.W[T][1]), cur.X[m], acc[T][m]);
            if constexpr (FIRST) {
                if (m == 3) {
                    if (T < 4) { if (wr == T) csa = mfma(cat(cur.W[T][0], cur.W[T][1]), ones, csa); }
                    else { if (wr == T - 4) csb = mfma(cat(cur.W[T][0], cur.W[T][1]), ones, csb); }
                }
            }
            if constexpr (decltype(READ)::value) { if (i < 18) load1(nxt, nsb, i); }
            if constexpr (decltype(DMA)::value) { if (i == 3 || i == 9 || i == 15 || i == 21) issue1(ks, dslot, (i - 3) / 6); }
            SCHED();
        }
        if constexpr (decltype(READ)::value) {
            wait_vm<decltype(VM)::value>();
            wait_lgkm0();
            bar();
        }
    };
    LAS f32x2* st = (LAS f32x2*)(lds + GU_STATS);
    LAS float* cs = (LAS float*)(lds + GU_CS);
    for (int t = 0; t < 28; t += 2) {
        step(f0, f1, t, T_{}, T_{}, std::integral_constant<int, 4 * (GU_LEAD - 2)>{});
        if (t == 0) {
            asm volatile("" : "+v"(sv[0]), "+v"(sv[1]), "+v"(sv[2]), "+v"(sv[3]), "+v"(cv));
#pragma unroll
            for (int i = 0; i < 4; ++i) { const f32x2 r = row_stats(sv[i]); if ((lane & 7) == 0) st[w * 32 + 8 * i + (lane >> 3)] = r; }
            if (!FIRST && tid < 224) cs[tid] = cv;
        }
        step(f1, f0, t + 1, T_{}, T_{}, std::integral_constant<int, 4 * (GU_LEAD - 2)>{});
    }
    if (GU_LEAD == 3) step(f0, f1, 28, T_{}, T_{}, std::integral_constant<int, 4>{});
    else step(f0, f1, 28, F_{}, T_{}, std::integral_constant<int, 4>{});
    step(f1, f0, 29, F_{}, T_{}, std::integral_constant<int, 0>{});
    step(f0, f1, 30, F_{}, T_{}, std::integral_constant<int, 0>{});
    step(f1, f0, 31, F_{}, F_{}, std::integral_constant<int, 0>{});
    STAMP(c, 3);

    if constexpr (FIRST) {
        const int ca = (wr < 3 ? 16 * wr : 112) + 56 * wc + 4 * g;
        const int cb = wr < 2 ? 112 + 16 * (wr + 1) + 56 * wc + 4 * g : (g >> 1) * 112 + 56 * wc + 48 + 4 * (g & 1);
        if (fr == 0) {
#pragma unroll
            for (int i = 0; i < 4; ++i) { cs[ca + i] = csa[i]; if (wr < 3) cs[cb + i] = csb[i]; }
        }
        wait_lgkm0();
        bar();
        if (pm == 0 && tid < 224) st4(mk_rsrc(ws + WS_CSG, 2 * FF * 4), ((tid >= 112 ? FF + tid - 112 : tid) + pn * 112) * 4, __float_as_uint(cs[tid]));
    }
    constexpr float LOG2E = 1.44269504088896341f;
    auto swiglu2 = [&](f32x2 a, f32x2 b, float rl, float r2) {
        const f32x2 x = a * rl;
        const f32x2 d = (f32x2){__builtin_amdgcn_exp2f(x.x), __builtin_amdgcn_exp2f(x.y)} + 1.f;
        const f32x2 pr = (a * b) * r2;
        return pr * (f32x2){__builtin_amdgcn_rcpf(d.x), __builtin_amdgcn_rcpf(d.y)};
    };
    f32x2 cg[3][2], cu[3][2], cm[2];
#pragma unroll
    for (int T = 0; T < 3; ++T)
#pragma unroll
        for (int i = 0; i < 2; ++i) { cg[T][i] = *(LAS const f32x2*)(cs + 56 * wc + 16 * T + 4 * g + 2 * i); cu[T][i] = *(LAS const f32x2*)(cs + 112 + 56 * wc + 16 * T + 4 * g + 2 * i); }
#pragma unroll
    for (int i = 0; i < 2; ++i) cm[i] = *(LAS const f32x2*)(cs + (g >> 1) * 112 + 56 * wc + 48 + 4 * (g & 1) + 2 * i);
    const __amdgpu_buffer_rsrc_t rh = mk_rsrc(ws + WS_H + L * 16 * MB, SEQ * FF * 2);
    const int kc0 = pn * 14 + wc * 7;
    unsigned P2[4][2], Mx[4];
#pragma unroll
    for (int m = 0; m < 4; ++m) {
        const f32x2 s = st[wr * 64 + 16 * m + fr];
        const float nm = -s.x, rl = -LOG2E * s.y, r2 = s.y * s.y;
        unsigned P[3][2];
#pragma unroll
        for (int T = 0; T < 3; ++T)
#pragma unroll
            for (int i = 0; i < 2; ++i) {
                const f32x2 a = (f32x2){acc[T][m][2 * i], acc[T][m][2 * i + 1]} + cg[T][i] * nm;
                const f32x2 b = (f32x2){acc[T + 3][m][2 * i], acc[T + 3][m][2 * i + 1]} + cu[T][i] * nm;
                const f32x2 h = swiglu2(a, b, rl, r2);
                P[T][i] = cvt_pk(h.x, h.y);
            }
        {
            const f32x2 v01 = (f32x2){acc[6][m][0], acc[6][m][1]} + cm[0] * nm, v23 = (f32x2){acc[6][m][2], acc[6][m][3]} + cm[1] * nm;
            float v0 = v01.x, v1 = v01.y, v2 = v23.x, v3 = v23.y;
            swap32f(v0, v2); swap32f(v1, v3);
            const f32x2 h = swiglu2((f32x2){v0, v1}, (f32x2){v2, v3}, rl, r2);
            Mx[m] = cvt_pk(h.x, h.y);
        }
        swap16(P[0][0], P[1][0]); swap16(P[0][1], P[1][1]);
        st16(rh, blk_off(pm * 256 + wr * 64 + 16 * m + fr, kc0 + 2 * (g & 1) + (g >> 1), FF / 8), (u32x4){P[0][0], P[0][1], P[1][0], P[1][1]});
        P2[m][0] = P[2][0]; P2[m][1] = P[2][1];
        if (m & 1) {
            swap16(P2[m - 1][0], P2[m][0]); swap16(P2[m - 1][1], P2[m][1]);
            st16(rh, blk_off(pm * 256 + wr * 64 + 16 * (m - 1 + (g & 1)) + fr, kc0 + 4 + (g >> 1), FF / 8), (u32x4){P2[m - 1][0], P2[m - 1][1], P2[m][0], P2[m][1]});
        }
    }
    {
        swap32(Mx[0], Mx[2]); swap32(Mx[1], Mx[3]);
        swap16(Mx[0], Mx[1]); swap16(Mx[2], Mx[3]);
        st16(rh, blk_off(pm * 256 + wr * 64 + 16 * g + fr, kc0 + 6, FF / 8), (u32x4){Mx[0], Mx[2], Mx[1], Mx[3]});
    }
    signal(c, F_GU + F_PER_LAYER * L, pm * 32 + pn);
    STAMP(c, 4); STAMP_FLUSH(c, 4 + 5 * L);
}

__global__ __launch_bounds__(512, 2) void k_mega(const bf16_t* __restrict__ x, const bf16_t* __restrict__ wqkv, const bf16_t* __restrict__ wo,
                                                 const bf16_t* __restrict__ wg, const bf16_t* __restrict__ wu, const bf16_t* __restrict__ wd,
                                                 char* __restrict__ out, char* __restrict__ ws) {
    extern __shared__ __attribute__((aligned(16))) char smem[];
    Ctx c;
    c.lds = (LAS char*)smem; c.ws = ws; c.tid = threadIdx.x; c.lane = threadIdx.x & 63; c.w = __builtin_amdgcn_readfirstlane(threadIdx.x >> 6); c.b = blockIdx.x;
    {
        const u64 e = __hip_atomic_load((const u64*)(ws + WS_EPOCH), __ATOMIC_RELAXED, __HIP_MEMORY_SCOPE_AGENT);
        const unsigned lo = __builtin_amdgcn_readfirstlane((unsigned)e), hi = __builtin_amdgcn_readfirstlane((unsigned)(e >> 32));
        c.tag = (((u64)hi << 32) | lo) + 1;
    }
    fresh(c); phase_ln(c, x, wqkv);
    fresh(c); phase_qkv<0>(c, wqkv);
    fresh(c); phase_attn<0>(c);
    fresh(c); phase_wo<0>(c, wo, x);
    fresh(c); phase_gateup<0>(c, wg, wu);
    fresh(c); phase_down<0>(c, wd, out);
    fresh(c); phase_qkv<1>(c, wqkv);
    fresh(c); phase_attn<1>(c);
    fresh(c); phase_wo<1>(c, wo, x);
    fresh(c); phase_gateup<1>(c, wg, wu);
    fresh(c); phase_down<1>(c, wd, out);
    if (c.b == 0 && c.tid == 0) __hip_atomic_store((u64*)(ws + WS_EPOCH), c.tag, __ATOMIC_RELAXED, __HIP_MEMORY_SCOPE_AGENT);
}

}

extern "C" void kernel_launch(void* const* d_in, const int* in_sizes, int n_in, void* d_out, int out_size, void* d_ws, size_t ws_size, hipStream_t stream) {
    (void)in_sizes; (void)n_in; (void)out_size; (void)ws_size;
    (void)hipFuncSetAttribute((const void*)k_mega, hipFuncAttributeMaxDynamicSharedMemorySize, LDS_BYTES);
    k_mega<<<256, 512, LDS_BYTES, stream>>>((const bf16_t*)d_in[0], (const bf16_t*)d_in[1], (const bf16_t*)d_in[2], (const bf16_t*)d_in[4],
                                            (const bf16_t*)d_in[5], (const bf16_t*)d_in[6], (char*)d_out, (char*)d_ws);
}
```

```cpp
#include <hip/hip_runtime.h>
#include <hip/hip_bf16.h>
#include <stdint.h>
#include <type_traits>

namespace {

typedef unsigned short bf16_t;
typedef unsigned long long u64;
typedef short bf16x8 __attribute__((ext_vector_type(8)));
typedef short bf16x4 __attribute__((ext_vector_type(4)));
typedef __bf16 bf16x2n __attribute__((ext_vector_type(2)));
typedef float f32x4 __attribute__((ext_vector_type(4)));
typedef float f32x2 __attribute__((ext_vector_type(2)));
typedef unsigned u32x4 __attribute__((ext_vector_type(4)));
typedef unsigned u32x2 __attribute__((ext_vector_type(2)));
#define LAS __attribute__((address_space(3)))
#define DI __device__ __forceinline__

constexpr int SEQ = 2048, DM = 1024, FF = 3584, NH = 32;
constexpr float LN_EPS = 1e-5f;

constexpr size_t MB = 1u << 20;
constexpr size_t WS_XN0 = 0;
constexpr size_t WS_PQ = 4 * MB;
constexpr size_t WS_O = 12 * MB;
constexpr size_t WS_X1 = 20 * MB;
constexpr size_t WS_X2 = 28 * MB;
constexpr size_t WS_H = 32 * MB;
constexpr size_t WS_KVP = 64 * MB;
constexpr size_t WS_ST1 = 72 * MB;
constexpr size_t WS_ST2 = 74 * MB;
constexpr size_t WS_CSQ = 75 * MB;
constexpr size_t WS_CSG = 76 * MB;
constexpr size_t WS_EX = 80 * MB;
constexpr size_t WS_FLAG = 96 * MB;
constexpr size_t WS_EPOCH = WS_FLAG + 16 * 256 * 8 + 1024;
constexpr int KVP_STRIDE = 1056;
enum { F_XN = 0, F_QKV = 1, F_ATT = 2, F_WO = 3, F_GU = 4, F_EX = 5, F_DN = 6, F_PER_LAYER = 6 };

DI unsigned cvt_pk(float lo, float hi) { f32x2 v = {lo, hi}; bf16x2n b = __builtin_convertvector(v, bf16x2n); return __builtin_bit_cast(unsigned, b); }
DI unsigned cvt_pk2(f32x2 v) { bf16x2n b = __builtin_convertvector(v, bf16x2n); return __builtin_bit_cast(unsigned, b); }
DI float bf_lo(unsigned u) { return __uint_as_float(u << 16); }
DI float bf_hi(unsigned u) { return __uint_as_float(u & 0xffff0000u); }
DI f32x2 phi2(f32x2 t) {
    const f32x2 u = t * 1.44269504088896341f;
    const f32x2 e = {__builtin_amdgcn_exp2f(__builtin_fminf(u.x, 0.f)), __builtin_amdgcn_exp2f(__builtin_fminf(u.y, 0.f))};
    const f32x2 m = {__builtin_fmaxf(t.x, 0.f), __builtin_fmaxf(t.y, 0.f)};
    return (m + e) + 1e-6f;
}
DI void swap16(unsigned& a, unsigned& b) { auto r = __builtin_amdgcn_permlane16_swap(a, b, false, false); a = r[0]; b = r[1]; }
DI void swap32(unsigned& a, unsigned& b) { auto r = __builtin_amdgcn_permlane32_swap(a, b, false, false); a = r[0]; b = r[1]; }
DI void swap16f(float& a, float& b) { unsigned x = __float_as_uint(a), y = __float_as_uint(b); swap16(x, y); a = __uint_as_float(x); b = __uint_as_float(y); }
DI void swap32f(float& a, float& b) { unsigned x = __float_as_uint(a), y = __float_as_uint(b); swap32(x, y); a = __uint_as_float(x); b = __uint_as_float(y); }
DI float xrow_sum(float x) {
    float a = x, b = x; swap16f(a, b); x = a + b;
    a = x; b = x; swap32f(a, b); return a + b;
}
template <int CTRL> DI float dpp(float x) { return __builtin_bit_cast(float, __builtin_amdgcn_update_dpp(0, __builtin_bit_cast(int, x), CTRL, 0xf, 0xf, true)); }
DI float sum4(float x) { x += dpp<0xB1>(x); x += dpp<0x4E>(x); return x; }
DI float sum8(float x) { x = sum4(x); x += dpp<0x141>(x); return x; }
template <bool SC1> DI void dma16(unsigned m0v, unsigned voff, __amdgpu_buffer_rsrc_t rs, unsigned soff) {
    if (SC1) asm volatile("s_mov_b32 m0, %0\n\ts_nop 0\n\tbuffer_load_dwordx4 %1, %2, %3 offen sc1 lds" ::"s"(m0v), "v"(voff), "s"(rs), "s"(soff) : "memory");
    else asm volatile("s_mov_b32 m0, %0\n\ts_nop 0\n\tbuffer_load_dwordx4 %1, %2, %3 offen lds" ::"s"(m0v), "v"(voff), "s"(rs), "s"(soff) : "memory");
}
DI void touch(unsigned m0v, unsigned voff, __amdgpu_buffer_rsrc_t rs, unsigned soff) {
    asm volatile("s_mov_b32 m0, %0\n\ts_nop 0\n\tbuffer_load_dword %1, %2, %3 offen lds" ::"s"(m0v), "v"(voff), "s"(rs), "s"(soff) : "memory");
}
template <int N> DI void wait_vm() { asm volatile("s_waitcnt vmcnt(%0)" ::"n"(N) : "memory"); }
DI void wait_lgkm0() { __builtin_amdgcn_s_waitcnt(0xC07F); }
DI void bar() { asm volatile("" ::: "memory"); __builtin_amdgcn_s_barrier(); asm volatile("" ::: "memory"); }
#define SCHED() __builtin_amdgcn_sched_barrier(0)
DI __amdgpu_buffer_rsrc_t mk_rsrc(const void* p, unsigned bytes) { return __builtin_amdgcn_make_buffer_rsrc(const_cast<void*>(p), 0, bytes, 0x00020000); }
DI bf16x8 lds_b128(LAS const char* p) { return *(LAS const bf16x8*)p; }
typedef short v4i16_t __attribute__((ext_vector_type(4)));
DI bf16x4 lds_tr(LAS const char* p) { return __builtin_amdgcn_ds_read_tr16_b64_v4i16((LAS v4i16_t*)p); }
DI bf16x8 cat(bf16x4 a, bf16x4 b) { return __builtin_shufflevector(a, b, 0, 1, 2, 3, 4, 5, 6, 7); }
DI f32x4 mfma(bf16x8 a, bf16x8 b, f32x4 c) { return __builtin_amdgcn_mfma_f32_16x16x32_bf16(a, b, c, 0, 0, 0); }
DI void st16(__amdgpu_buffer_rsrc_t rs, unsigned off, u32x4 v) { __builtin_amdgcn_raw_buffer_store_b128(v, rs, off, 0, 16); }
DI void st8(__amdgpu_buffer_rsrc_t rs, unsigned off, u32x2 v) { __builtin_amdgcn_raw_buffer_store_b64(v, rs, off, 0, 16); }
DI void st4(__amdgpu_buffer_rsrc_t rs, unsigned off, unsigned v) { __builtin_amdgcn_raw_buffer_store_b32(v, rs, off, 0, 16); }
DI u32x4 ld16(__amdgpu_buffer_rsrc_t rs, unsigned off) { return __builtin_amdgcn_raw_buffer_load_b128(rs, off, 0, 16); }
DI unsigned ld4(__amdgpu_buffer_rsrc_t rs, unsigned off) { return __builtin_amdgcn_raw_buffer_load_b32(rs, off, 0, 16); }
DI f32x2 row_stats(u32x4 v) {
    const float m0 = __uint_as_float(v[0]), q0 = __uint_as_float(v[1]), m1 = __uint_as_float(v[2]), q1 = __uint_as_float(v[3]);
    const float mean = sum8(m0 + m1) * (1.f / 16.f);
    const float d0 = m0 - mean, d1 = m1 - mean;
    const float q = sum8(q0 + q1 + 64.f * (d0 * d0 + d1 * d1));
    return (f32x2){mean, __builtin_amdgcn_rsqf(q * (1.f / 1024.f) + LN_EPS)};
}
DI unsigned blk_off(unsigned r, unsigned kc, unsigned KC) { return (((r >> 6) * KC + kc) << 10) + ((r & 63) << 4); }

template <int NT, int NC> DI int stage_of(int t, int r) {
    constexpr int N = NT - 1;
    const int cstar = r + NC * ((N - 1 - r) / NC);
    int c = t - 2;
    if (c >= cstar) c = t - 1;
    else if ((c & (NC - 1)) == r) c += NC;
    int ks = t == 0 ? 0 : (t == 1 ? r : c) + 1;
    asm volatile("" : "+s"(ks));
    return ks;
}
typedef std::integral_constant<bool, true> T_;
typedef std::integral_constant<bool, false> F_;

struct Ctx {
    LAS char* lds; char* ws; int tid, lane, w, b; u64 tag;
};
#define STAMP(c, i) do { } while (0)
#define STAMPC(c, i) do { } while (0)
#define STAMP_FLUSH(c, ph) do { } while (0)
constexpr int LDS_DUMMY = 144 * 1024;
constexpr int LDS_BYTES = LDS_DUMMY + 1024;

DI void fresh(Ctx& c) {
    int t = c.tid;
    asm volatile("" : "+v"(t), "+s"(c.b), "+s"(c.w), "+s"(c.ws));
    c.tid = t; c.lane = t & 63;
}
DI void signal(const Ctx& c, int set, int idx) {
    wait_vm<0>();
    bar();
    if (c.tid == 0) __hip_atomic_store((u64*)(c.ws + WS_FLAG) + set * 256 + idx, c.tag, __ATOMIC_RELAXED, __HIP_MEMORY_SCOPE_AGENT);
}
DI void wait_flags(const Ctx& c, int set, int idx0, int n, int stride = 1) {
    const u64* f = (const u64*)(c.ws + WS_FLAG) + set * 256 + idx0 + (c.lane < n ? c.lane : 0) * stride;
    const u64 t0 = __builtin_amdgcn_s_memrealtime();
    while (!__all(__hip_atomic_load(f, __ATOMIC_RELAXED, __HIP_MEMORY_SCOPE_AGENT) == c.tag)) {
        __builtin_amdgcn_s_sleep(1);
        if (__builtin_amdgcn_s_memrealtime() - t0 > 2000000ull) break;
    }
}

DI void wait_flags_at(const Ctx& c, int set, int idx) {
    const u64* f = (const u64*)(c.ws + WS_FLAG) + set * 256 + idx;
    const u64 t0 = __builtin_amdgcn_s_memrealtime();
    while (!__all(__hip_atomic_load(f, __ATOMIC_RELAXED, __HIP_MEMORY_SCOPE_AGENT) == c.tag)) {
        __builtin_amdgcn_s_sleep(1);
        if (__builtin_amdgcn_s_memrealtime() - t0 > 2000000ull) break;
    }
}

DI void phase_ln(Ctx& c, const bf16_t* __restrict__ x) {
    const int r0 = c.b * 8, lane = c.lane, w = c.w;
    STAMP(c, 0);
    const u32x4* src = (const u32x4*)(x + (size_t)(r0 + w) * DM);
    const u32x4 a = src[lane], b = src[lane + 64];
    float v[16];
#pragma unroll
    for (int j = 0; j < 4; ++j) { v[2 * j] = bf_lo(a[j]); v[2 * j + 1] = bf_hi(a[j]); v[8 + 2 * j] = bf_lo(b[j]); v[8 + 2 * j + 1] = bf_hi(b[j]); }
    float s = 0.f;
#pragma unroll
    for (int j = 0; j < 16; ++j) s += v[j];
    float t = sum8(s); t += dpp<0x140>(t); t = xrow_sum(t);
    const float mean = t * (1.f / 1024.f);
    float q = 0.f;
#pragma unroll
    for (int j = 0; j < 16; ++j) { v[j] -= mean; q += v[j] * v[j]; }
    q = sum8(q); q += dpp<0x140>(q); q = xrow_sum(q);
    const float rs = __builtin_amdgcn_rsqf(q * (1.f / 1024.f) + LN_EPS);
    u32x4 oa, ob;
#pragma unroll
    for (int j = 0; j < 4; ++j) { oa[j] = cvt_pk(v[2 * j] * rs, v[2 * j + 1] * rs); ob[j] = cvt_pk(v[8 + 2 * j] * rs, v[8 + 2 * j + 1] * rs); }
    *(LAS u32x4*)(c.lds + (w * 129 + lane) * 16) = oa;
    *(LAS u32x4*)(c.lds + (w * 129 + lane + 64) * 16) = ob;
    wait_lgkm0();
    bar();
    const __amdgpu_buffer_rsrc_t ro = mk_rsrc(c.ws + WS_XN0, SEQ * DM * 2);
#pragma unroll
    for (int i = 0; i < 2; ++i) {
        const int idx = c.tid + 512 * i, kc = idx >> 3, rl = idx & 7;
        st16(ro, blk_off(r0 + rl, kc, 128), *(LAS const u32x4*)(c.lds + (rl * 129 + kc) * 16));
    }
    signal(c, F_XN, c.b);
    STAMP(c, 4); STAMP_FLUSH(c, 0);
}

template <int NSEG_, int NSUB_, int S_, int KSPAN_, int KFULL_, int PITCH_, int SEGSTEP_, int NC_>
struct SplitCfg {
    static constexpr int NSEG = NSEG_, NSUB = NSUB_, S = S_, KSPAN = KSPAN_, KFULL = KFULL_, PITCH = PITCH_, SEGSTEP = SEGSTEP_, NC = NC_;
    static constexpr int ABYTES = 2 * 4 * NSUB * 1024, BBYTES = NSUB * NSEG * 4096, STAGE = ABYTES + BBYTES;
    static constexpr int NT = KSPAN / (32 * NSUB);
    static constexpr int KC = KFULL / 8;
    static constexpr int PA = 2 * NSUB, PB = NSUB * NSEG, P = PA + PB;
    static constexpr int RING = S * STAGE;
};

struct NoSide {
    static constexpr int N = 0;
    DI void issue(int, unsigned) const {}
    DI void use(LAS const char*) {}
};
constexpr int LDS_SIDE = 128 * 1024;
template <class C, class Side>
DI void split_loader(Ctx& c, int lw, int cls, const char* A, unsigned a_bytes, unsigned a_base, const char* B, unsigned b_bytes, unsigned b_base, Side& side) {
    const __amdgpu_buffer_rsrc_t ra = mk_rsrc(A, a_bytes), rb = mk_rsrc(B, b_bytes);
    const unsigned ldsb = (unsigned)(size_t)c.lds;
    const int lane = c.lane;
    const unsigned va = lane * 16;
    const unsigned rl = lane >> 3, sl = lane & 7;
    const unsigned vb = (8 * (rl >> 2) + (rl & 3)) * C::PITCH + (2 * ((sl >> 1) ^ ((rl >> 1) & 3)) + (sl & 1)) * 16;
    const unsigned a_src = a_base + (lw >> 1) * (C::KC * 1024) + (lw & 1) * (C::PA * 1024);
    const unsigned a_dst = (lw >> 1) * (4 * C::NSUB * 1024) + (lw & 1) * (C::PA * 1024);
    const unsigned b_src = b_base + (16 * (lw >> 1) + 4 * (lw & 1)) * C::PITCH;
    const unsigned b_dst = C::ABYTES + lw * 1024;
    constexpr int PB = C::PB + Side::N, P = C::PA + PB;
    auto issueA = [&](int tt) {
        const unsigned t = stage_of<C::NT, C::NC>(tt, cls);
        const unsigned sb = ldsb + (unsigned)(tt % C::S) * C::STAGE;
#pragma unroll
        for (int p = 0; p < C::PA; ++p) dma16<true>(sb + a_dst + p * 1024, va, ra, a_src + (t * 4 * C::NSUB + p) * 1024);
    };
    auto issueB = [&](int tt) {
        const unsigned t = stage_of<C::NT, C::NC>(tt, cls);
        const unsigned sb = ldsb + (unsigned)(tt % C::S) * C::STAGE;
#pragma unroll
        for (int u = 0; u < C::NSUB; ++u)
#pragma unroll
            for (int g = 0; g < C::NSEG; ++g) dma16<false>(sb + b_dst + (u * C::NSEG + g) * 4096, vb, rb, b_src + (t * C::NSUB + u) * 32 * C::PITCH + g * C::SEGSTEP);
        side.issue(tt, ldsb + LDS_SIDE + ((tt & 3) * 4 + lw) * 1024);
    };
    auto use = [&](int tt) { side.use(c.lds + LDS_SIDE + ((tt & 3) * 4 + lw) * 1024); };
    auto issue = [&](int tt) { issueA(tt); issueB(tt); };
    static_assert(C::S == 3 || C::S == 4, "ring depth");
    (void)PB;
    issueB(0); issueB(1);
    bar();
    issueA(0); issueA(1);
    wait_vm<C::PA>();
    bar();
    use(0); use(1);
    issue(2);
    wait_vm<P>();
    bar();
    int nx = 3;
    if (C::S == 4) { issue(3); nx = 4; }
    issue(nx); ++nx;
    for (; nx < C::NT; ++nx) {
        wait_vm<P*(C::S - 2)>();
        bar();
        use(nx - C::S + 1);
        issue(nx);
    }
    wait_vm<P*(C::S - 2)>(); bar(); use(C::NT - C::S + 1);
    if (C::S == 4) { wait_vm<P>(); bar(); use(C::NT - 2); }
    wait_vm<0>(); bar(); use(C::NT - 1);
}

template <class C> struct Frags { bf16x8 X[4]; bf16x4 W[C::NSEG][2][2]; };

template <class C, int TMASK>
struct Consumer {
    LAS char* lds; unsigned xa, wa[2];
    DI Consumer(LAS char* lds_, int cw, int lane) : lds(lds_) {
        const int wm = cw & 1, wn = cw >> 1;
        const int g = lane >> 4, fr = lane & 15, q = (lane & 15) >> 2, p = lane & 3;
        xa = wm * (4 * C::NSUB * 1024) + g * 1024 + fr * 16;
        const unsigned rho = 16 * (g >> 1) + 4 * (g & 1) + q, xr = 2 * (g & 1) + (q >> 1);
#pragma unroll
        for (int dt = 0; dt < 2; ++dt) wa[dt] = C::ABYTES + rho * 128 + (((2 * wn + dt) ^ xr) << 5) + (p >> 1) * 16 + (p & 1) * 8;
    }
    static constexpr int NR = 4 + 4 * C::NSEG;
    static constexpr int NM = 8 * C::NSEG;
    DI void load1(Frags<C>& f, unsigned sb, int u, int i) const {
        if (i >= 2 && i < 6) f.X[i - 2] = lds_b128(lds + sb + xa + u * 4096 + (i - 2) * 256);
        else {
            const int k = i < 2 ? i : i - 4, s = k >> 2, dt = (k >> 1) & 1, h = k & 1;
            f.W[s][dt][h] = lds_tr(lds + sb + wa[dt] + (u * C::NSEG + s) * 4096 + h * 1024);
        }
    }
    DI static bf16x8 wfrag(const Frags<C>& f, int s, int dt) { return cat(f.W[s][dt][0], f.W[s][dt][1]); }
    DI void mma1(const Frags<C>& f, f32x4 (&acc)[C::NSEG][2][4], int i) const {
        const int s = i >> 3, dt = (i >> 2) & 1, mt = i & 3;
        if ((TMASK >> s) & 1) acc[s][dt][mt] = mfma(wfrag(f, s, dt), f.X[mt], acc[s][dt][mt]);
        else acc[s][dt][mt] = mfma(f.X[mt], wfrag(f, s, dt), acc[s][dt][mt]);
    }
};

template <class C, int TMASK, class Extra>
DI void split_consumer(Ctx& c, f32x4 (&acc)[C::NSEG][2][4], Extra extra) {
    typedef Consumer<C, TMASK> K;
    const K k(c.lds, c.w, c.lane);
    auto sub = [&](const Frags<C>& cur, Frags<C>& nxt, unsigned sb, int u, auto READ) {
#pragma unroll
        for (int i = 0; i < K::NM; ++i) {
            k.mma1(cur, acc, i);
            if constexpr (decltype(READ)::value) { if (i < K::NR) k.load1(nxt, sb, u, i); }
            SCHED();
        }
    };
    Frags<C> f0, f1;
    bar();
    STAMP(c, 2);
#pragma unroll
    for (int i = 0; i < K::NR; ++i) k.load1(f0, 0, 0, i);
    wait_lgkm0();
    unsigned slot = 0;
    static_assert(C::NSUB == 2, "NSUB");
    for (int t = 0; t < C::NT - 1; ++t) {
        const unsigned sb = slot * C::STAGE;
        slot = (slot + 1 == C::S) ? 0 : slot + 1;
        sub(f0, f1, sb, 1, T_{});
        extra(f0, t);
        wait_lgkm0();
        bar();
        sub(f1, f0, slot * C::STAGE, 0, T_{});
        extra(f1, t);
        wait_lgkm0();
    }
    sub(f0, f1, slot * C::STAGE, 1, T_{});
    extra(f0, C::NT - 1);
    wait_lgkm0();
    sub(f1, f0, 0, 0, F_{});
    extra(f1, C::NT - 1);
    STAMP(c, 3);
}

template <class C, int TMASK>
DI void split_consumer_stage(Ctx& c, f32x4 (&acc)[C::NSEG][2][4]) {
    typedef Consumer<C, TMASK> K;
    const K k(c.lds, c.w, c.lane);
    struct SF { Frags<C> u[C::NSUB]; };
    constexpr int NM = K::NM * C::NSUB, NR = K::NR * C::NSUB;
    auto step = [&](const SF& cur, SF& nxt, unsigned sb, auto READ, auto BARR) {
#pragma unroll
        for (int i = 0; i < NM; ++i) {
            k.mma1(cur.u[i / K::NM], acc, i % K::NM);
            if constexpr (decltype(READ)::value) {
                if constexpr (NR <= NM * 3 / 4) {
                    if (i < NR) k.load1(nxt.u[i / K::NR], sb, i / K::NR, i % K::NR);
                } else {
                    if (2 * i < NR) k.load1(nxt.u[(2 * i) / K::NR], sb, (2 * i) / K::NR, (2 * i) % K::NR);
                    if (2 * i + 1 < NR) k.load1(nxt.u[(2 * i + 1) / K::NR], sb, (2 * i + 1) / K::NR, (2 * i + 1) % K::NR);
                }
            }
            SCHED();
        }
        wait_lgkm0();
        if constexpr (decltype(BARR)::value) bar();
    };
    SF f0, f1;
    bar();
    STAMP(c, 2);
#pragma unroll
    for (int i = 0; i < NR; ++i) k.load1(f0.u[i / K::NR], 0, i / K::NR, i % K::NR);
    wait_lgkm0();
    bar();
    static_assert(C::NT % 2 == 0 && C::S == 4, "stage loop");
    for (int t = 0; t < C::NT - 2; t += 2) {
        step(f0, f1, ((t + 1) & 3) * C::STAGE, T_{}, T_{});
        step(f1, f0, ((t + 2) & 3) * C::STAGE, T_{}, T_{});
    }
    step(f0, f1, ((C::NT - 1) & 3) * C::STAGE, T_{}, F_{});
    step(f1, f0, 0, F_{}, F_{});
    STAMP(c, 3);
}

typedef SplitCfg<3, 2, 3, DM, DM, 3 * DM * 2, 2048, 4> CfgQKV;

DI int wo_fidx(int b) { const int xcd = b & 7, j = b >> 3, pm = 4 * (xcd & 3) + (j & 3), pn = 8 * (xcd >> 2) + (j >> 2); return (pm >> 1) * 32 + (pm & 1) * 16 + pn; }
template <int L>
DI void phase_qkv(Ctx& c, const bf16_t* __restrict__ wqkv) {
    typedef CfgQKV C;
    constexpr bool LNFUSE = L == 1;
    LAS char* lds = c.lds; char* ws = c.ws;
    const int lane = c.lane, w = c.w;
    const int xcd = c.b & 7, j = c.b >> 3;
    const int pm = 8 * (xcd & 1) + (j & 7), hp = 4 * (xcd >> 1) + (j >> 3);
    const int cls = (j + (j >> 3)) & 3;
    STAMP(c, 0);
    if (w >= 4) {
        NoSide side;
        split_loader<C>(c, w - 4, cls, ws + (L == 0 ? WS_XN0 : WS_X2), SEQ * DM * 2, pm * 2 * (C::KC * 1024), (const char*)wqkv, DM * 3 * DM * 2, hp * 128, side);
        if (LNFUSE) bar();
        bar();
        bar();
        signal(c, F_QKV + F_PER_LAYER * L, hp * 16 + pm);
        return;
    }
    if (w == 0) wait_flags(c, L == 0 ? F_XN : F_DN, pm * 16, 16);
    bar();
    STAMP(c, 1);
    const int wm = w & 1, hh = w >> 1, g = lane >> 4, fr = lane & 15;
    u32x4 sv[4], cv[3];
    if (LNFUSE) {
        const __amdgpu_buffer_rsrc_t rs = mk_rsrc(ws + WS_ST2, SEQ * 128), rc = mk_rsrc(ws + WS_CSQ, 3 * DM * 64);
#pragma unroll
        for (int i = 0; i < 4; ++i) sv[i] = ld16(rs, (pm * 128 + w * 32 + 8 * i + (lane >> 3)) * 128 + (lane & 7) * 16);
#pragma unroll
        for (int i = 0; i < 3; ++i) {
            const int cc = 48 * w + 16 * i + (lane >> 2);
            cv[i] = ld16(rc, ((cc >> 6) * DM + hp * 64 + (cc & 63)) * 64 + (lane & 3) * 16);
        }
    }
    f32x4 acc[3][2][4];
#pragma unroll
    for (int s = 0; s < 3; ++s)
#pragma unroll
        for (int d = 0; d < 2; ++d)
#pragma unroll
            for (int m = 0; m < 4; ++m) acc[s][d][m] = (f32x4){0.f, 0.f, 0.f, 0.f};
    const bf16x8 ones = {0x3F80, 0x3F80, 0x3F80, 0x3F80, 0x3F80, 0x3F80, 0x3F80, 0x3F80};
    f32x4 csum[3] = {{0.f, 0.f, 0.f, 0.f}, {0.f, 0.f, 0.f, 0.f}, {0.f, 0.f, 0.f, 0.f}};
    auto extra = [&](const Frags<C>& f, int t) {
        if constexpr (!LNFUSE) {
            if (stage_of<C::NT, C::NC>(t, cls) == pm) {
                typedef Consumer<C, 1> K;
                if (wm == 0) { csum[0] = mfma(K::wfrag(f, 0, 0), ones, csum[0]); csum[1] = mfma(K::wfrag(f, 0, 1), ones, csum[1]); csum[2] = mfma(K::wfrag(f, 1, 0), ones, csum[2]); }
                else { csum[0] = mfma(K::wfrag(f, 1, 1), ones, csum[0]); csum[1] = mfma(K::wfrag(f, 2, 0), ones, csum[1]); csum[2] = mfma(K::wfrag(f, 2, 1), ones, csum[2]); }
            }
        }
    };
    split_consumer<C, 1>(c, acc, extra);

    LAS f32x2* st = (LAS f32x2*)(lds + C::RING);
    LAS float* cs = (LAS float*)(lds + C::RING + 1024);
    if (LNFUSE) {
#pragma unroll
        for (int i = 0; i < 4; ++i) { const f32x2 r = row_stats(sv[i]); if ((lane & 7) == 0) st[w * 32 + 8 * i + (lane >> 3)] = r; }
#pragma unroll
        for (int i = 0; i < 3; ++i) {
            const float cc = sum4((__uint_as_float(cv[i][0]) + __uint_as_float(cv[i][1])) + (__uint_as_float(cv[i][2]) + __uint_as_float(cv[i][3])));
            if ((lane & 3) == 0) cs[48 * w + 16 * i + (lane >> 2)] = cc;
        }
        wait_lgkm0();
        bar();
    } else if (fr == 0) {
        const __amdgpu_buffer_rsrc_t rc = mk_rsrc(ws + WS_CSQ, 3 * DM * 64);
#pragma unroll
        for (int k = 0; k < 3; ++k) {
            const int f = 3 * wm + k, s = f >> 1, dt = f & 1;
#pragma unroll
            for (int i = 0; i < 4; ++i) st4(rc, (s * DM + hp * 64 + hh * 32 + 16 * dt + 4 * g + i) * 64 + pm * 4, __float_as_uint(csum[k][i]));
        }
    }
    const int head = hp * 2 + hh;
    bf16x8 kf[2][2], vf[2][2];
    {
        f32x2 nm[4][2], rs[4][2];
        if (LNFUSE) {
#pragma unroll
            for (int mt = 0; mt < 4; ++mt)
#pragma unroll
                for (int i = 0; i < 2; ++i) {
                    const f32x4 s = *(LAS const f32x4*)(st + wm * 64 + 16 * mt + 4 * g + 2 * i);
                    nm[mt][i] = (f32x2){-s[0], -s[2]}; rs[mt][i] = (f32x2){s[1], s[3]};
                }
        }
#pragma unroll
        for (int dt = 0; dt < 2; ++dt) {
            float ck = 0.f, cvv = 0.f;
            if (LNFUSE) { ck = cs[64 + hh * 32 + 16 * dt + fr]; cvv = cs[128 + hh * 32 + 16 * dt + fr]; }
#pragma unroll
            for (int sp = 0; sp < 2; ++sp) {
                u32x4 ku, vu;
#pragma unroll
                for (int h2 = 0; h2 < 2; ++h2) {
                    const int mt = 2 * sp + h2;
#pragma unroll
                    for (int i = 0; i < 2; ++i) {
                        f32x2 tk = {acc[1][dt][mt][2 * i], acc[1][dt][mt][2 * i + 1]}, tv = {acc[2][dt][mt][2 * i], acc[2][dt][mt][2 * i + 1]};
                        if (LNFUSE) { tk = (tk + nm[mt][i] * ck) * rs[mt][i]; tv = (tv + nm[mt][i] * cvv) * rs[mt][i]; }
                        ku[2 * h2 + i] = cvt_pk2(phi2(tk)); vu[2 * h2 + i] = cvt_pk2(tv);
                    }
                }
                kf[dt][sp] = __builtin_bit_cast(bf16x8, ku); vf[dt][sp] = __builtin_bit_cast(bf16x8, vu);
            }
        }
    }
    f32x4 kv[2][2], z[2];
#pragma unroll
    for (int dt = 0; dt < 2; ++dt) {
        z[dt] = mfma(kf[dt][0], ones, (f32x4){0.f, 0.f, 0.f, 0.f});
        z[dt] = mfma(kf[dt][1], ones, z[dt]);
#pragma unroll
        for (int et = 0; et < 2; ++et) {
            kv[dt][et] = mfma(kf[dt][0], vf[et][0], (f32x4){0.f, 0.f, 0.f, 0.f});
            kv[dt][et] = mfma(kf[dt][1], vf[et][1], kv[dt][et]);
        }
    }
    const __amdgpu_buffer_rsrc_t rq = mk_rsrc(ws + WS_PQ + L * 4 * MB, SEQ * DM * 2);
#pragma unroll
    for (int mt = 0; mt < 4; ++mt) {
        float nmean = 0.f, rstd = 1.f;
        if (LNFUSE) { const f32x2 s = st[wm * 64 + 16 * mt + fr]; nmean = -s.x; rstd = s.y; }
        unsigned pk[2][2];
#pragma unroll
        for (int dt = 0; dt < 2; ++dt)
#pragma unroll
            for (int i = 0; i < 2; ++i) {
                f32x2 t = {acc[0][dt][mt][2 * i], acc[0][dt][mt][2 * i + 1]};
                if (LNFUSE) t = (t + *(LAS const f32x2*)(cs + hh * 32 + 16 * dt + 4 * g + 2 * i) * nmean) * rstd;
                pk[dt][i] = cvt_pk2(phi2(t));
            }
        swap16(pk[0][0], pk[1][0]); swap16(pk[0][1], pk[1][1]);
        const int ch = ((g & 1) << 1) | (g >> 1);
        st16(rq, blk_off(pm * 128 + wm * 64 + 16 * mt + fr, head * 4 + ch, 128), (u32x4){pk[0][0], pk[0][1], pk[1][0], pk[1][1]});
    }
    bar();
    LAS f32x4* red = (LAS f32x4*)(lds + hh * 8192);
    if (wm == 1) {
#pragma unroll
        for (int dt = 0; dt < 2; ++dt) {
#pragma unroll
            for (int et = 0; et < 2; ++et) red[(dt * 2 + et) * 64 + lane] = kv[dt][et];
            red[(4 + dt) * 64 + lane] = z[dt];
        }
        wait_lgkm0();
    }
    bar();
    if (wm == 0) {
        const __amdgpu_buffer_rsrc_t rk = mk_rsrc(ws + WS_KVP + L * 4 * MB, 16 * NH * KVP_STRIDE * 4);
        const unsigned dst = (pm * NH + head) * KVP_STRIDE * 4;
#pragma unroll
        for (int dt = 0; dt < 2; ++dt) {
#pragma unroll
            for (int et = 0; et < 2; ++et) st16(rk, dst + ((dt * 2 + et) * 64 + lane) * 16, __builtin_bit_cast(u32x4, kv[dt][et] + red[(dt * 2 + et) * 64 + lane]));
            const f32x4 zz = z[dt] + red[(4 + dt) * 64 + lane];
            if (fr == 0) st16(rk, dst + (1024 + 16 * dt + 4 * g) * 4, __builtin_bit_cast(u32x4, zz));
        }
    }
    signal(c, F_QKV + F_PER_LAYER * L, hp * 16 + pm);
    STAMP(c, 4); STAMP_FLUSH(c, 1 + 5 * L);
}

template <int L>
DI void phase_attn(Ctx& c, const bf16_t* __restrict__ wo) {
    LAS char* sm = c.lds;
    char* ws = c.ws;
    const int tid = c.tid, lane = c.lane, w = c.w, g = lane >> 4, fr = lane & 15;
    const int xcd = c.b & 7, j = c.b >> 3;
    const int head = 4 * xcd + (j & 3), pb = j >> 2;
    STAMP(c, 0);
    if (w >= 4) {
        bar();
        if (L == 0 && w == 7) touch((unsigned)(size_t)c.lds + LDS_DUMMY, lane * 128, mk_rsrc(wo, DM * DM * 2), c.b * 8192);
        bar(); bar();
        return;
    }
    if (w == 0) wait_flags(c, F_QKV + F_PER_LAYER * L, (head >> 1) * 16, 16);
    bar();
    STAMP(c, 1);
    const __amdgpu_buffer_rsrc_t rk = mk_rsrc(ws + WS_KVP + L * 4 * MB, 16 * NH * KVP_STRIDE * 4), rq = mk_rsrc(ws + WS_PQ + L * 4 * MB, SEQ * DM * 2);
    f32x4 s = {0.f, 0.f, 0.f, 0.f}, zs = {0.f, 0.f, 0.f, 0.f};
    bf16x8 qf[4];
    {
        f32x4 p[16], pz[16];
#pragma unroll
        for (int i = 0; i < 16; ++i) p[i] = __builtin_bit_cast(f32x4, ld16(rk, (i * NH + head) * (KVP_STRIDE * 4) + tid * 16));
        if (tid < 8) {
#pragma unroll
            for (int i = 0; i < 16; ++i) pz[i] = __builtin_bit_cast(f32x4, ld16(rk, (i * NH + head) * (KVP_STRIDE * 4) + 4096 + tid * 16));
        }
#pragma unroll
        for (int mt = 0; mt < 4; ++mt) qf[mt] = __builtin_bit_cast(bf16x8, ld16(rq, blk_off(pb * 256 + w * 64 + 16 * mt + fr, head * 4 + g, 128)));
#pragma unroll
        for (int i = 0; i < 16; ++i) s += p[i];
        if (tid < 8) {
#pragma unroll
            for (int i = 0; i < 16; ++i) zs += pz[i];
        }
    }
    {
        const int dt = w >> 1, et = w & 1;
        *(LAS u32x2*)(sm + (16 * et + fr) * 80 + (16 * dt + 4 * g) * 2) = (u32x2){cvt_pk(s[0], s[1]), cvt_pk(s[2], s[3])};
        if (tid < 8) *(LAS u32x2*)(sm + 2560 + tid * 8) = (u32x2){cvt_pk(zs[0], zs[1]), cvt_pk(zs[2], zs[3])};
    }
    wait_lgkm0();
    bar();
    bf16x8 kf[2];
#pragma unroll
    for (int et = 0; et < 2; ++et) kf[et] = lds_b128(sm + (16 * et + fr) * 80 + g * 16);
    const bf16x8 zf = lds_b128(sm + 2560 + g * 16);
    const f32x4 zero = {0.f, 0.f, 0.f, 0.f};
    const __amdgpu_buffer_rsrc_t ro = mk_rsrc(ws + WS_O + L * 4 * MB, SEQ * DM * 2);
#pragma unroll
    for (int mt = 0; mt < 4; ++mt) {
        const f32x4 den = mfma(zf, qf[mt], zero);
        const f32x4 n0 = mfma(kf[0], qf[mt], zero), n1 = mfma(kf[1], qf[mt], zero);
        const float r = __builtin_amdgcn_rcpf(den[0] + 1e-6f);
        unsigned a0 = cvt_pk(n0[0] * r, n0[1] * r), a1 = cvt_pk(n0[2] * r, n0[3] * r);
        unsigned b0 = cvt_pk(n1[0] * r, n1[1] * r), b1 = cvt_pk(n1[2] * r, n1[3] * r);
        swap16(a0, b0); swap16(a1, b1);
        const int ch = ((g & 1) << 1) | (g >> 1);
        st16(ro, blk_off(pb * 256 + w * 64 + 16 * mt + fr, head * 4 + ch, 128), (u32x4){a0, a1, b0, b1});
    }
    signal(c, F_ATT + F_PER_LAYER * L, pb * 32 + head);
    STAMP(c, 4); STAMP_FLUSH(c, 2 + 5 * L);
}

template <bool OUT_RM>
DI void epi_n1024(Ctx& c, f32x4 (&acc)[2][4], const u32x4 (&rv)[4], int pm, int pn, char* out, char* stats) {
    const int lane = c.lane, w = c.w, wm = w & 1, wn = w >> 1, g = lane >> 4, fr = lane & 15;
    const int kc = pn * 8 + wn * 4 + (((g & 1) << 1) | (g >> 1));
    const __amdgpu_buffer_rsrc_t ro = mk_rsrc(out, SEQ * DM * 2);
    f32x2 part[4];
#pragma unroll
    for (int mt = 0; mt < 4; ++mt) {
        float v[8];
#pragma unroll
        for (int i = 0; i < 4; ++i) { float a = acc[0][mt][i], b = acc[1][mt][i]; swap16f(a, b); v[i] = a; v[4 + i] = b; }
        u32x4 o;
#pragma unroll
        for (int i = 0; i < 4; ++i) o[i] = cvt_pk(v[2 * i] + bf_lo(rv[mt][i]), v[2 * i + 1] + bf_hi(rv[mt][i]));
        const int row = pm * 128 + wm * 64 + 16 * mt + fr;
        if (OUT_RM) st16(ro, row * (DM * 2) + kc * 16, o);
        else {
            st16(ro, blk_off(row, kc, 128), o);
            float s = 0.f;
#pragma unroll
            for (int i = 0; i < 4; ++i) { v[2 * i] = bf_lo(o[i]); v[2 * i + 1] = bf_hi(o[i]); s += v[2 * i] + v[2 * i + 1]; }
            const float m = xrow_sum(s) * (1.f / 32.f);
            float q = 0.f;
#pragma unroll
            for (int i = 0; i < 8; ++i) q += (v[i] - m) * (v[i] - m);
            part[mt] = (f32x2){m, xrow_sum(q)};
        }
    }
    if (!OUT_RM) {
        bar();
        LAS f32x2* sp = (LAS f32x2*)c.lds;
        if (wn == 1 && g == 0) {
#pragma unroll
            for (int mt = 0; mt < 4; ++mt) sp[wm * 64 + 16 * mt + fr] = part[mt];
        }
        wait_lgkm0();
        bar();
        if (wn == 0 && g == 0) {
#pragma unroll
            for (int mt = 0; mt < 4; ++mt) {
                const f32x2 o = sp[wm * 64 + 16 * mt + fr];
                const float d = part[mt].x - o.x;
                const f32x2 r = {0.5f * (part[mt].x + o.x), part[mt].y + o.y + 16.f * d * d};
                st8(mk_rsrc(stats, SEQ * 128), (pm * 128 + wm * 64 + 16 * mt + fr) * 128 + pn * 8, __builtin_bit_cast(u32x2, r));
            }
        }
    }
}

typedef SplitCfg<1, 2, 4, DM, DM, DM * 2, 0, 4> CfgWO;
template <int L>
DI void phase_wo(Ctx& c, const bf16_t* __restrict__ wo, const bf16_t* __restrict__ x) {
    typedef CfgWO C;
    char* ws = c.ws;
    const int lane = c.lane, w = c.w;
    const int xcd = c.b & 7, j = c.b >> 3;
    const int pm = 4 * (xcd & 3) + (j & 3), pn = 8 * (xcd >> 2) + (j >> 2);
    const int fidx = wo_fidx(c.b);
    STAMP(c, 0);
    if (w >= 4) {
        NoSide side;
        split_loader<C>(c, w - 4, (j + (j >> 2)) & 3, ws + WS_O + L * 4 * MB, SEQ * DM * 2, pm * 2 * (C::KC * 1024), (const char*)wo, DM * DM * 2, pn * 128, side);
        bar(); bar();
        signal(c, F_WO + F_PER_LAYER * L, fidx);
        return;
    }
    if (w == 0) wait_flags(c, F_ATT + F_PER_LAYER * L, (pm >> 1) * 32, 32);
    bar();
    STAMP(c, 1);
    const int wm = w & 1, wn = w >> 1, g = lane >> 4, fr = lane & 15;
    const int kc = pn * 8 + wn * 4 + (((g & 1) << 1) | (g >> 1));
    u32x4 rv[4];
#pragma unroll
    for (int mt = 0; mt < 4; ++mt) {
        const int row = pm * 128 + wm * 64 + 16 * mt + fr;
        rv[mt] = L == 0 ? *(const u32x4*)((const char*)x + (size_t)row * (DM * 2) + kc * 16) : ld16(mk_rsrc(ws + WS_X2, SEQ * DM * 2), blk_off(row, kc, 128));
    }
    f32x4 acc[1][2][4];
#pragma unroll
    for (int d = 0; d < 2; ++d)
#pragma unroll
        for (int m = 0; m < 4; ++m) acc[0][d][m] = (f32x4){0.f, 0.f, 0.f, 0.f};
    split_consumer_stage<C, 1>(c, acc);
    epi_n1024<false>(c, acc[0], rv, pm, pn, ws + WS_X1 + L * 4 * MB, ws + WS_ST1 + L * MB);
    signal(c, F_WO + F_PER_LAYER * L, fidx);
    STAMP(c, 4); STAMP_FLUSH(c, 3 + 5 * L);
}

typedef SplitCfg<1, 2, 4, FF, FF, DM * 2, 0, 4> CfgDN;
template <int L>
DI void phase_down(Ctx& c, const bf16_t* __restrict__ wd, char* __restrict__ out) {
    typedef CfgDN C;
    constexpr bool OUT_RM = L == 1;
    char* ws = c.ws;
    const int lane = c.lane, w = c.w;
    const int xcd = c.b & 7, j = c.b >> 3;
    const int pm = 4 * (xcd & 3) + (j & 3), pn = 8 * (xcd >> 2) + (j >> 2);
    STAMP(c, 0);
    if (w >= 4) {
        NoSide side;
        split_loader<C>(c, w - 4, (j + (j >> 2)) & 3, ws + WS_H + L * 16 * MB, SEQ * FF * 2, pm * 2 * (C::KC * 1024), (const char*)wd, FF * DM * 2, pn * 128, side);
        if (!OUT_RM) { bar(); bar(); signal(c, F_DN, pm * 16 + pn); }
        return;
    }
    if (w == 0) wait_flags(c, F_GU + F_PER_LAYER * L, (pm >> 1) * 32, 32);
    bar();
    STAMP(c, 1);
    const int wm = w & 1, wn = w >> 1, g = lane >> 4, fr = lane & 15;
    const int kc = pn * 8 + wn * 4 + (((g & 1) << 1) | (g >> 1));
    u32x4 rv[4];
#pragma unroll
    for (int mt = 0; mt < 4; ++mt) rv[mt] = ld16(mk_rsrc(ws + WS_X1 + L * 4 * MB, SEQ * DM * 2), blk_off(pm * 128 + wm * 64 + 16 * mt + fr, kc, 128));
    f32x4 acc[1][2][4];
#pragma unroll
    for (int d = 0; d < 2; ++d)
#pragma unroll
        for (int m = 0; m < 4; ++m) acc[0][d][m] = (f32x4){0.f, 0.f, 0.f, 0.f};
    split_consumer_stage<C, 1>(c, acc);
    epi_n1024<OUT_RM>(c, acc[0], rv, pm, pn, OUT_RM ? out : ws + WS_X2, ws + WS_ST2);
    if (!OUT_RM) signal(c, F_DN, pm * 16 + pn);
    STAMP(c, 4); STAMP_FLUSH(c, 5 + 5 * L);
}

#define GU_LEAD 3
#define GU_NC 4
#define GU_TOUCH_T 8
constexpr int GU_S = 4;
constexpr int GU_GOFF = 16384, GU_UOFF = 16384 + 7168 + 16, GU_STAGE = GU_UOFF + 7168;
constexpr int GU_RING = GU_S * GU_STAGE;
constexpr int GU_STATS = GU_RING, GU_CS = GU_STATS + 2048;
static_assert(GU_CS + 1024 <= LDS_DUMMY && CfgDN::RING <= LDS_DUMMY && CfgQKV::RING + 4096 <= LDS_SIDE && LDS_SIDE + 16384 <= LDS_DUMMY + 4096, "LDS map");
struct GuFrags { bf16x8 X[4]; bf16x4 W[7][2]; };

template <int L>
DI void phase_gateup(Ctx& c, const bf16_t* __restrict__ wg, const bf16_t* __restrict__ wu, const bf16_t* __restrict__ wd) {
    constexpr bool FIRST = L == 0;
    LAS char* lds = c.lds; char* ws = c.ws;
    const int tid = c.tid, lane = c.lane, w = c.w;
    const int xcd = c.b & 7, jj = c.b >> 3;
    const int pm = 4 * (xcd & 1) + (jj & 3), pn = 8 * (xcd >> 1) + (jj >> 2);
    const int wr = w >> 1, wc = w & 1, g = lane >> 4, fr = lane & 15, q = fr >> 2, p = lane & 3;
    STAMP(c, 0);

    const __amdgpu_buffer_rsrc_t ra = mk_rsrc(ws + WS_X1 + L * 4 * MB, SEQ * DM * 2);
    const __amdgpu_buffer_rsrc_t rg = mk_rsrc(wg, w <= 6 ? DM * FF * 2 : 0), ru = mk_rsrc(wu, w >= 1 ? DM * FF * 2 : 0);
    const unsigned ldsb = (unsigned)(size_t)lds;
    const unsigned va = lane * 16;
    auto bpat = [&](int piece) { const unsigned i = 64 * piece + lane, rho = i / 14, cc = i % 14; const unsigned k = (rho & 0x13) | ((rho & 8) >> 1) | ((rho & 4) << 1); return k * (FF * 2) + cc * 16; };
    const unsigned vg = bpat(w <= 6 ? w : 0), vu = bpat(w >= 1 ? w - 1 : 0);
    const unsigned a_src = (pm * 4 + (w >> 1)) * (128 * 1024) + (w & 1) * 2048, a_dst = (w >> 1) * 4096 + (w & 1) * 2048;
    const unsigned g_dst0 = w <= 6 ? GU_GOFF + w * 1024 : LDS_DUMMY, g_str = w <= 6 ? GU_STAGE : 0;
    const unsigned u_dst0 = w >= 1 ? GU_UOFF + (w - 1) * 1024 : LDS_DUMMY, u_str = w >= 1 ? GU_STAGE : 0;
    const unsigned b_src = pn * 224;
    const int cls = ((jj & 3) + (jj >> 2)) & (GU_NC - 1);
    auto issue1 = [&](int ks, unsigned slot, int i) {
        if (i < 2) dma16<true>(ldsb + slot * GU_STAGE + a_dst + i * 1024, va, ra, a_src + (ks * 4 + i) * 1024);
        else if (i == 2) dma16<false>(ldsb + g_dst0 + slot * g_str, vg, rg, b_src + ks * (32 * FF * 2));
        else dma16<false>(ldsb + u_dst0 + slot * u_str, vu, ru, b_src + ks * (32 * FF * 2));
    };
    auto issue = [&](int t, int i0, int i1) {
        const int ks = stage_of<32, GU_NC>(t, cls);
#pragma unroll
        for (int i = 0; i < 4; ++i) if (i >= i0 && i < i1) issue1(ks, t & 3, i);
    };
    issue(0, 2, 4); issue(1, 2, 4);
    if (w == 0) wait_flags(c, F_WO + F_PER_LAYER * L, pm * 32, 32);
    bar();
    STAMP(c, 1);
    issue(0, 0, 2); issue(1, 0, 2);
    u32x4 sv[4]; float cv = 0.f;
    {
        const __amdgpu_buffer_rsrc_t rs = mk_rsrc(ws + WS_ST1 + L * MB, SEQ * 128), rc = mk_rsrc(ws + WS_CSG, 2 * FF * 4);
        const unsigned so = (pm * 256 + w * 32 + (lane >> 3)) * 128 + (lane & 7) * 16;
#pragma unroll
        for (int i = 0; i < 4; ++i) asm volatile("buffer_load_dwordx4 %0, %1, %2, 0 offen sc1" : "=v"(sv[i]) : "v"(so + i * 1024), "s"(rs) : "memory");
        const int cc = tid < 224 ? tid : 0;
        const unsigned co = ((cc >= 112 ? FF + cc - 112 : cc) + pn * 112) * 4;
        if (!FIRST) asm volatile("buffer_load_dword %0, %1, %2, 0 offen sc1" : "=v"(cv) : "v"(co), "s"(rc) : "memory");
    }
    constexpr int NX = FIRST ? 4 : 5;

    const unsigned xa = wr * 4096 + g * 1024 + fr * 16;
    const unsigned rho0 = 16 * (g >> 1) + 4 * (g & 1) + q;
    const unsigned wa = (rho0 * 14 + 7 * wc + (p >> 1)) * 16 + (p & 1) * 8;
    const unsigned wm_ = (rho0 * 14 + 7 * wc + 6) * 16 + (p & 1) * 8 + ((p >> 1) ? GU_UOFF : GU_GOFF);
    auto tile_addr = [&](int T) { return T < 3 ? wa + GU_GOFF + T * 32 : (T < 6 ? wa + GU_UOFF + (T - 3) * 32 : wm_); };
    const unsigned ca0 = tile_addr(wr), ca1 = tile_addr(wr < 3 ? wr + 4 : 3);
    auto load1 = [&](GuFrags& f, unsigned sb, int i) {
        if (i >= 2 && i < 6) f.X[i - 2] = lds_b128(lds + sb + xa + (i - 2) * 256);
        else {
            const int k = i < 2 ? i : i - 4, T = k >> 1, h = k & 1;
            LAS const char* pw = T < 3 ? lds + sb + wa + GU_GOFF + T * 32 : (T < 6 ? lds + sb + wa + GU_UOFF + (T - 3) * 32 : lds + sb + wm_);
            f.W[T][h] = lds_tr(pw + h * 1792);
        }
    };

    wait_vm<2 + NX>();
    bar();
    STAMP(c, 2);
    GuFrags f0, f1;
#pragma unroll
    for (int i = 0; i < 18; ++i) load1(f0, 0, i);
    issue(2, 0, 4);
    if (GU_LEAD == 4) issue(3, 0, 4);
    wait_vm<NX + 4 * (GU_LEAD - 2)>();
    wait_lgkm0();
    bar();

    f32x4 acc[7][4];
#pragma unroll
    for (int T = 0; T < 7; ++T)
#pragma unroll
        for (int m = 0; m < 4; ++m) acc[T][m] = (f32x4){0.f, 0.f, 0.f, 0.f};

    f32x4 csa = {0.f, 0.f, 0.f, 0.f}, csb = {0.f, 0.f, 0.f, 0.f};
    bf16x8 ones = {0x3F80, 0x3F80, 0x3F80, 0x3F80, 0x3F80, 0x3F80, 0x3F80, 0x3F80};
    if (FIRST) asm volatile("" : "+v"(ones));
    static_assert(GU_LEAD == 3, "the column-sum reads use the slot of the current stage: it must not be the DMA destination");
    auto step = [&](const GuFrags& cur, GuFrags& nxt, int t, auto DMA, auto READ, auto VM) {
        const unsigned nsb = ((t + 1) & (GU_S - 1)) * GU_STAGE;
        const unsigned dslot = (t + GU_LEAD) & (GU_S - 1);
        const int ks = stage_of<32, GU_NC>(t + GU_LEAD, cls);
        const unsigned csb_ = (t & (GU_S - 1)) * GU_STAGE;
        bf16x4 wcs[2][2];
#pragma unroll
        for (int i = 0; i < 28; ++i) {
            const int T = i >> 2, m = i & 3;
            acc[T][m] = mfma(cat(cur.W[T][0], cur.W[T][1]), cur.X[m], acc[T][m]);
            if constexpr (FIRST) {
                if (i >= 18 && i < 22) wcs[(i - 18) >> 1][i & 1] = lds_tr(lds + csb_ + (i < 20 ? ca0 : ca1) + (i & 1) * 1792);
                if (i == 26) csa = mfma(cat(wcs[0][0], wcs[0][1]), ones, csa);
                if (i == 27) csb = mfma(cat(wcs[1][0], wcs[1][1]), ones, csb);
            }
            if constexpr (decltype(READ)::value) { if (i < 18) load1(nxt, nsb, i); }
            if constexpr (decltype(DMA)::value) { if (i == 3 || i == 9 || i == 15 || i == 21) issue1(ks, dslot, (i - 3) / 6); }
            SCHED();
        }
        if constexpr (FIRST && decltype(DMA)::value) {
            if (t == GU_TOUCH_T && w < 4) { const unsigned l = w * 64 + lane; touch(ldsb + LDS_DUMMY, l < 224 ? l * 128 : 0x7fffff00u, mk_rsrc(wd, FF * DM * 2), c.b * 28672); }
        }
        if constexpr (decltype(READ)::value) {
            wait_vm<decltype(VM)::value>();
            wait_lgkm0();
            bar();
        }
    };
    LAS f32x2* st = (LAS f32x2*)(lds + GU_STATS);
    LAS float* cs = (LAS float*)(lds + GU_CS);
    for (int t = 0; t < 28; t += 2) {
        step(f0, f1, t, T_{}, T_{}, std::integral_constant<int, 4 * (GU_LEAD - 2)>{});
        if (t == 0) {
            asm volatile("" : "+v"(sv[0]), "+v"(sv[1]), "+v"(sv[2]), "+v"(sv[3]), "+v"(cv));
#pragma unroll
            for (int i = 0; i < 4; ++i) { const f32x2 r = row_stats(sv[i]); if ((lane & 7) == 0) st[w * 32 + 8 * i + (lane >> 3)] = r; }
            if (!FIRST && tid < 224) cs[tid] = cv;
        }
        step(f1, f0, t + 1, T_{}, T_{}, std::integral_constant<int, 4 * (GU_LEAD - 2)>{});
    }
    if (GU_LEAD == 3) step(f0, f1, 28, T_{}, T_{}, std::integral_constant<int, 4>{});
    else step(f0, f1, 28, F_{}, T_{}, std::integral_constant<int, 4>{});
    step(f1, f0, 29, F_{}, T_{}, std::integral_constant<int, 0>{});
    step(f0, f1, 30, F_{}, T_{}, std::integral_constant<int, 0>{});
    step(f1, f0, 31, F_{}, F_{}, std::integral_constant<int, 0>{});
    STAMP(c, 3);

    if constexpr (FIRST) {
        const int ca = (wr < 3 ? 16 * wr : 112) + 56 * wc + 4 * g;
        const int cb = wr < 2 ? 112 + 16 * (wr + 1) + 56 * wc + 4 * g : (g >> 1) * 112 + 56 * wc + 48 + 4 * (g & 1);
        if (fr == 0) {
#pragma unroll
            for (int i = 0; i < 4; ++i) { cs[ca + i] = csa[i]; if (wr < 3) cs[cb + i] = csb[i]; }
        }
        wait_lgkm0();
        bar();
        if (pm == 0 && tid < 224) st4(mk_rsrc(ws + WS_CSG, 2 * FF * 4), ((tid >= 112 ? FF + tid - 112 : tid) + pn * 112) * 4, __float_as_uint(cs[tid]));
    }
    constexpr float LOG2E = 1.44269504088896341f;
    auto swiglu2 = [&](f32x2 a, f32x2 b, float rl, float r2) {
        const f32x2 x = a * rl;
        const f32x2 d = (f32x2){__builtin_amdgcn_exp2f(x.x), __builtin_amdgcn_exp2f(x.y)} + 1.f;
        const f32x2 pr = (a * b) * r2;
        return pr * (f32x2){__builtin_amdgcn_rcpf(d.x), __builtin_amdgcn_rcpf(d.y)};
    };
    f32x2 cg[3][2], cu[3][2], cm[2];
#pragma unroll
    for (int T = 0; T < 3; ++T)
#pragma unroll
        for (int i = 0; i < 2; ++i) { cg[T][i] = *(LAS const f32x2*)(cs + 56 * wc + 16 * T + 4 * g + 2 * i); cu[T][i] = *(LAS const f32x2*)(cs + 112 + 56 * wc + 16 * T + 4 * g + 2 * i); }
#pragma unroll
    for (int i = 0; i < 2; ++i) cm[i] = *(LAS const f32x2*)(cs + (g >> 1) * 112 + 56 * wc + 48 + 4 * (g & 1) + 2 * i);
    const __amdgpu_buffer_rsrc_t rh = mk_rsrc(ws + WS_H + L * 16 * MB, SEQ * FF * 2);
    const int kc0 = pn * 14 + wc * 7;
    unsigned P2[4][2], Mx[4];
#pragma unroll
    for (int m = 0; m < 4; ++m) {
        const f32x2 s = st[wr * 64 + 16 * m + fr];
        const float nm = -s.x, rl = -LOG2E * s.y, r2 = s.y * s.y;
        unsigned P[3][2];
#pragma unroll
        for (int T = 0; T < 3; ++T)
#pragma unroll
            for (int i = 0; i < 2; ++i) {
                const f32x2 a = (f32x2){acc[T][m][2 * i], acc[T][m][2 * i + 1]} + cg[T][i] * nm;
                const f32x2 b = (f32x2){acc[T + 3][m][2 * i], acc[T + 3][m][2 * i + 1]} + cu[T][i] * nm;
                const f32x2 h = swiglu2(a, b, rl, r2);
                P[T][i] = cvt_pk(h.x, h.y);
            }
        {
            const f32x2 v01 = (f32x2){acc[6][m][0], acc[6][m][1]} + cm[0] * nm, v23 = (f32x2){acc[6][m][2], acc[6][m][3]} + cm[1] * nm;
            float v0 = v01.x, v1 = v01.y, v2 = v23.x, v3 = v23.y;
            swap32f(v0, v2); swap32f(v1, v3);
            const f32x2 h = swiglu2((f32x2){v0, v1}, (f32x2){v2, v3}, rl, r2);
            Mx[m] = cvt_pk(h.x, h.y);
        }
        swap16(P[0][0], P[1][0]); swap16(P[0][1], P[1][1]);
        st16(rh, blk_off(pm * 256 + wr * 64 + 16 * m + fr, kc0 + 2 * (g & 1) + (g >> 1), FF / 8), (u32x4){P[0][0], P[0][1], P[1][0], P[1][1]});
        P2[m][0] = P[2][0]; P2[m][1] = P[2][1];
        if (m & 1) {
            swap16(P2[m - 1][0], P2[m][0]); swap16(P2[m - 1][1], P2[m][1]);
            st16(rh, blk_off(pm * 256 + wr * 64 + 16 * (m - 1 + (g & 1)) + fr, kc0 + 4 + (g >> 1), FF / 8), (u32x4){P2[m - 1][0], P2[m - 1][1], P2[m][0], P2[m][1]});
        }
    }
    {
        swap32(Mx[0], Mx[2]); swap32(Mx[1], Mx[3]);
        swap16(Mx[0], Mx[1]); swap16(Mx[2], Mx[3]);
        st16(rh, blk_off(pm * 256 + wr * 64 + 16 * g + fr, kc0 + 6, FF / 8), (u32x4){Mx[0], Mx[2], Mx[1], Mx[3]});
    }
    signal(c, F_GU + F_PER_LAYER * L, pm * 32 + pn);
    STAMP(c, 4); STAMP_FLUSH(c, 4 + 5 * L);
}

__global__ __launch_bounds__(512, 2) void k_mega(const bf16_t* __restrict__ x, const bf16_t* __restrict__ wqkv, const bf16_t* __restrict__ wo,
                                                 const bf16_t* __restrict__ wg, const bf16_t* __restrict__ wu, const bf16_t* __restrict__ wd,
                                                 char* __restrict__ out, char* __restrict__ ws) {
    extern __shared__ __attribute__((aligned(16))) char smem[];
    Ctx c;
    c.lds = (LAS char*)smem; c.ws = ws; c.tid = threadIdx.x; c.lane = threadIdx.x & 63; c.w = __builtin_amdgcn_readfirstlane(threadIdx.x >> 6); c.b = blockIdx.x;
    {
        const u64 e = __hip_atomic_load((const u64*)(ws + WS_EPOCH), __ATOMIC_RELAXED, __HIP_MEMORY_SCOPE_AGENT);
        const unsigned lo = __builtin_amdgcn_readfirstlane((unsigned)e), hi = __builtin_amdgcn_readfirstlane((unsigned)(e >> 32));
        c.tag = (((u64)hi << 32) | lo) + 1;
    }
    fresh(c); phase_ln(c, x);
    fresh(c); phase_qkv<0>(c, wqkv);
    fresh(c); phase_attn<0>(c, wo);
    fresh(c); phase_wo<0>(c, wo, x);
    fresh(c); phase_gateup<0>(c, wg, wu, wd);
    fresh(c); phase_down<0>(c, wd, out);
    fresh(c); phase_qkv<1>(c, wqkv);
    fresh(c); phase_attn<1>(c, wo);
    fresh(c); phase_wo<1>(c, wo, x);
    fresh(c); phase_gateup<1>(c, wg, wu, wd);
    fresh(c); phase_down<1>(c, wd, out);
    if (c.b == 0 && c.tid == 0) __hip_atomic_store((u64*)(ws + WS_EPOCH), c.tag, __ATOMIC_RELAXED, __HIP_MEMORY_SCOPE_AGENT);
}

}

extern "C" void kernel_launch(void* const* d_in, const int* in_sizes, int n_in, void* d_out, int out_size, void* d_ws, size_t ws_size, hipStream_t stream) {
    (void)in_sizes; (void)n_in; (void)out_size; (void)ws_size;
    (void)hipFuncSetAttribute((const void*)k_mega, hipFuncAttributeMaxDynamicSharedMemorySize, LDS_BYTES);
    k_mega<<<256, 512, LDS_BYTES, stream>>>((const bf16_t*)d_in[0], (const bf16_t*)d_in[1], (const bf16_t*)d_in[2], (const bf16_t*)d_in[4],
                                            (const bf16_t*)d_in[5], (const bf16_t*)d_in[6], (char*)d_out, (char*)d_ws);
}
```

```cpp
#include <hip/hip_runtime.h>
#include <hip/hip_bf16.h>
#include <stdint.h>
#include <type_traits>

namespace {

typedef unsigned short bf16_t;
typedef unsigned long long u64;
typedef short bf16x8 __attribute__((ext_vector_type(8)));
typedef short bf16x4 __attribute__((ext_vector_type(4)));
typedef __bf16 bf16x2n __attribute__((ext_vector_type(2)));
typedef float f32x4 __attribute__((ext_vector_type(4)));
typedef float f32x2 __attribute__((ext_vector_type(2)));
typedef unsigned u32x4 __attribute__((ext_vector_type(4)));
typedef unsigned u32x2 __attribute__((ext_vector_type(2)));
#define LAS __attribute__((address_space(3)))
#define GAS __attribute__((address_space(1)))
#define DI __device__ __forceinline__

constexpr int SEQ = 2048, DM = 1024, FF = 3584, NH = 32;
constexpr float LN_EPS = 1e-5f;

constexpr size_t MB = 1u << 20;
constexpr size_t WS_STX = 0;
constexpr size_t WS_PQ = 4 * MB;
constexpr size_t WS_O = 8 * MB;
constexpr size_t WS_X1 = 12 * MB;
constexpr size_t WS_X2 = 16 * MB;
constexpr size_t WS_H = 20 * MB;
constexpr size_t WS_KVP = 36 * MB;
constexpr size_t WS_ST1 = 38 * MB;
constexpr size_t WS_ST2 = 39 * MB;
constexpr size_t WS_CSQ = 40 * MB;
constexpr size_t WS_CSG = 41 * MB;
constexpr size_t WS_FLAG = 42 * MB;
constexpr int FLAG_STRIDE = 16, FLAG_BYTES = 10 * 256 * 128;
constexpr size_t WS_EPOCH = WS_FLAG + FLAG_BYTES + 1024;
constexpr int KVP_BYTES = 2048 + 128;
enum { F_XN = 0, F_DN = 1, F_QKV = 2, F_ATT = 3, F_WO = 4, F_GU = 5, F_PER_LAYER = 4 };

DI unsigned cvt_pk(float lo, float hi) { f32x2 v = {lo, hi}; bf16x2n b = __builtin_convertvector(v, bf16x2n); return __builtin_bit_cast(unsigned, b); }
DI unsigned cvt_pk2(f32x2 v) { bf16x2n b = __builtin_convertvector(v, bf16x2n); return __builtin_bit_cast(unsigned, b); }
DI float bf_lo(unsigned u) { return __uint_as_float(u << 16); }
DI float bf_hi(unsigned u) { return __uint_as_float(u & 0xffff0000u); }
DI f32x2 phi2l(f32x2 u) {
    const f32x2 e = {__builtin_amdgcn_exp2f(__builtin_fminf(u.x, 0.f)), __builtin_amdgcn_exp2f(__builtin_fminf(u.y, 0.f))};
    const f32x2 m = {__builtin_fmaxf(u.x, 0.f), __builtin_fmaxf(u.y, 0.f)};
    return m * 0.693147180559945309f + (e + 1e-6f);
}
constexpr float LOG2E = 1.44269504088896341f;
DI void swap16(unsigned& a, unsigned& b) { auto r = __builtin_amdgcn_permlane16_swap(a, b, false, false); a = r[0]; b = r[1]; }
DI void swap32(unsigned& a, unsigned& b) { auto r = __builtin_amdgcn_permlane32_swap(a, b, false, false); a = r[0]; b = r[1]; }
DI void swap16f(float& a, float& b) { unsigned x = __float_as_uint(a), y = __float_as_uint(b); swap16(x, y); a = __uint_as_float(x); b = __uint_as_float(y); }
DI void swap32f(float& a, float& b) { unsigned x = __float_as_uint(a), y = __float_as_uint(b); swap32(x, y); a = __uint_as_float(x); b = __uint_as_float(y); }
DI float xrow_sum(float x) {
    float a = x, b = x; swap16f(a, b); x = a + b;
    a = x; b = x; swap32f(a, b); return a + b;
}
template <int CTRL> DI float dpp(float x) { return __builtin_bit_cast(float, __builtin_amdgcn_update_dpp(0, __builtin_bit_cast(int, x), CTRL, 0xf, 0xf, true)); }
DI float sum4(float x) { x += dpp<0xB1>(x); x += dpp<0x4E>(x); return x; }
DI float sum8(float x) { x = sum4(x); x += dpp<0x141>(x); return x; }
template <bool SC1> DI void dma16(unsigned m0v, unsigned voff, __amdgpu_buffer_rsrc_t rs, unsigned soff) {
    if (SC1) asm volatile("s_mov_b32 m0, %0\n\ts_nop 0\n\tbuffer_load_dwordx4 %1, %2, %3 offen sc1 lds" ::"s"(m0v), "v"(voff), "s"(rs), "s"(soff) : "memory");
    else asm volatile("s_mov_b32 m0, %0\n\ts_nop 0\n\tbuffer_load_dwordx4 %1, %2, %3 offen lds" ::"s"(m0v), "v"(voff), "s"(rs), "s"(soff) : "memory");
}
DI void touch(unsigned m0v, unsigned voff, __amdgpu_buffer_rsrc_t rs, unsigned soff) {
    asm volatile("s_mov_b32 m0, %0\n\ts_nop 0\n\tbuffer_load_dword %1, %2, %3 offen lds" ::"s"(m0v), "v"(voff), "s"(rs), "s"(soff) : "memory");
}
template <int N> DI void wait_vm() { asm volatile("s_waitcnt vmcnt(%0)" ::"n"(N) : "memory"); }
DI void wait_lgkm0() { __builtin_amdgcn_s_waitcnt(0xC07F); }
DI void bar() { asm volatile("" ::: "memory"); __builtin_amdgcn_s_barrier(); asm volatile("" ::: "memory"); }
#define SCHED() __builtin_amdgcn_sched_barrier(0)
DI __amdgpu_buffer_rsrc_t mk_rsrc(const void* p, unsigned bytes) { return __builtin_amdgcn_make_buffer_rsrc(const_cast<void*>(p), 0, bytes, 0x00020000); }
DI bf16x8 lds_b128(LAS const char* p) { return *(LAS const bf16x8*)p; }
typedef short v4i16_t __attribute__((ext_vector_type(4)));
DI bf16x4 lds_tr(LAS const char* p) { return __builtin_amdgcn_ds_read_tr16_b64_v4i16((LAS v4i16_t*)p); }
DI bf16x8 cat(bf16x4 a, bf16x4 b) { return __builtin_shufflevector(a, b, 0, 1, 2, 3, 4, 5, 6, 7); }
DI f32x4 mfma(bf16x8 a, bf16x8 b, f32x4 c) { return __builtin_amdgcn_mfma_f32_16x16x32_bf16(a, b, c, 0, 0, 0); }
DI void st16(__amdgpu_buffer_rsrc_t rs, unsigned off, u32x4 v) { __builtin_amdgcn_raw_buffer_store_b128(v, rs, off, 0, 16); }
DI void st8(__amdgpu_buffer_rsrc_t rs, unsigned off, u32x2 v) { __builtin_amdgcn_raw_buffer_store_b64(v, rs, off, 0, 16); }
DI void st4(__amdgpu_buffer_rsrc_t rs, unsigned off, unsigned v) { __builtin_amdgcn_raw_buffer_store_b32(v, rs, off, 0, 16); }
DI u32x4 ld16(__amdgpu_buffer_rsrc_t rs, unsigned off) { return __builtin_amdgcn_raw_buffer_load_b128(rs, off, 0, 16); }
DI f32x4 row_stats(const u32x4 (&v)[4]) {
    float m0 = 0.f, m1 = 0.f;
#pragma unroll
    for (int i = 0; i < 4; ++i) { m0 += __uint_as_float(v[i][0]); m1 += __uint_as_float(v[i][2]); }
    m0 = xrow_sum(m0) * (1.f / 16.f); m1 = xrow_sum(m1) * (1.f / 16.f);
    float q0 = 0.f, q1 = 0.f;
#pragma unroll
    for (int i = 0; i < 4; ++i) {
        const float d0 = __uint_as_float(v[i][0]) - m0, d1 = __uint_as_float(v[i][2]) - m1;
        q0 += __uint_as_float(v[i][1]) + 64.f * d0 * d0; q1 += __uint_as_float(v[i][3]) + 64.f * d1 * d1;
    }
    q0 = xrow_sum(q0); q1 = xrow_sum(q1);
    return (f32x4){m0, __builtin_amdgcn_rsqf(q0 * (1.f / 1024.f) + LN_EPS), m1, __builtin_amdgcn_rsqf(q1 * (1.f / 1024.f) + LN_EPS)};
}
DI unsigned stats_off(int row0, int lane, int i) { return ((4 * i + (lane >> 4)) * SEQ + row0 + 2 * (lane & 15)) * 8; }
DI unsigned blk_off(unsigned r, unsigned kc, unsigned KC) { return (((r >> 6) * KC + kc) << 10) + ((r & 63) << 4); }

template <int NT, int NC> DI int stage_of(int t, int r) {
    constexpr int N = NT - 1;
    const int cstar = r + NC * ((N - 1 - r) / NC);
    int c = t - 2;
    if (c >= cstar) c = t - 1;
    else if ((c & (NC - 1)) == r) c += NC;
    int ks = t == 0 ? 0 : (t == 1 ? r : c) + 1;
    if (t >= NT) ks = NT;
    asm volatile("" : "+s"(ks));
    return ks;
}
typedef std::integral_constant<bool, true> T_;
typedef std::integral_constant<bool, false> F_;

struct Ctx {
    LAS char* lds; char* ws; int tid, lane, w, b; u64 tag;
};
constexpr int LDS_DUMMY = 144 * 1024;
constexpr int LDS_BYTES = LDS_DUMMY + 1024;

DI int lane_id() { int l; asm volatile("v_mbcnt_lo_u32_b32 %0, -1, 0\n\tv_mbcnt_hi_u32_b32 %0, -1, %0" : "=v"(l)); return l; }
DI void fresh(Ctx& c) {
    asm volatile("" : "+s"(c.b), "+s"(c.w), "+s"(c.ws));
    c.lane = lane_id(); c.tid = c.w * 64 + c.lane;
}
DI void signal(const Ctx& c, int set, int idx) {
    wait_vm<0>();
    bar();
    if (c.tid == 0) __hip_atomic_store((GAS u64*)(c.ws + WS_FLAG) + (set * 256 + idx) * FLAG_STRIDE, c.tag, __ATOMIC_RELAXED, __HIP_MEMORY_SCOPE_AGENT);
}
DI void wait_flags(const Ctx& c, int set, int idx0, int n) {
    const GAS u64* f = (const GAS u64*)(c.ws + WS_FLAG) + (set * 256 + idx0 + (c.lane < n ? c.lane : 0)) * FLAG_STRIDE;
    const u64 t0 = __builtin_amdgcn_s_memrealtime();
    while (!__all(__hip_atomic_load(f, __ATOMIC_RELAXED, __HIP_MEMORY_SCOPE_AGENT) == c.tag)) {
        __builtin_amdgcn_s_sleep(1);
        if (__builtin_amdgcn_s_memrealtime() - t0 > 2000000ull) break;
    }
}

template <int NSEG_, int NSUB_, int S_, int KSPAN_, int KFULL_, int PITCH_, int SEGSTEP_, int NC_, bool AROW_ = false>
struct SplitCfg {
    static constexpr int NSEG = NSEG_, NSUB = NSUB_, S = S_, KSPAN = KSPAN_, KFULL = KFULL_, PITCH = PITCH_, SEGSTEP = SEGSTEP_, NC = NC_;
    static constexpr bool AROW = AROW_;
    static constexpr int ABYTES = 2 * 4 * NSUB * 1024, BBYTES = NSUB * NSEG * 4096, STAGE = ABYTES + BBYTES;
    static constexpr int NT = KSPAN / (32 * NSUB);
    static constexpr int KC = KFULL / 8;
    static constexpr int PA = 2 * NSUB, PB = NSUB * NSEG, P = PA + PB;
    static constexpr int RING = S * STAGE;
};

struct NoSide {
    static constexpr int NPRE = 0;
    DI void pre() {}
    DI void landed() {}
    DI void use(int) {}
};
template <class C, bool HAS_G = true, class Side>
DI void split_loader(Ctx& c, int lw, int cls, const char* A, unsigned a_bytes, unsigned a_base, const char* B, unsigned b_bytes, unsigned b_base, Side& side) {
    const __amdgpu_buffer_rsrc_t ra = mk_rsrc(A, a_bytes), rb = mk_rsrc(B, b_bytes);
    const unsigned ldsb = (unsigned)(size_t)c.lds;
    const int lane = c.lane;
    const unsigned va = lane * 16;
    const unsigned rl = lane >> 3, sl = lane & 7;
    const unsigned vb = (8 * (rl >> 2) + (rl & 3)) * C::PITCH + (2 * ((sl >> 1) ^ ((rl >> 1) & 3)) + (sl & 1)) * 16;
    const unsigned a_src = C::AROW ? a_base + lw * 32 * (C::KFULL * 2) : a_base + (lw >> 1) * (C::KC * 1024) + (lw & 1) * (C::PA * 1024);
    const unsigned a_dst = C::AROW ? lw * 4096 : (lw >> 1) * (4 * C::NSUB * 1024) + (lw & 1) * (C::PA * 1024);
    const unsigned var0 = rl * (C::KFULL * 2) + (sl ^ (rl >> 1)) * 16, var1 = rl * (C::KFULL * 2) + (sl ^ (4 + (rl >> 1))) * 16;
    const unsigned b_src = b_base + (16 * (lw >> 1) + 4 * (lw & 1)) * C::PITCH;
    const unsigned b_dst = C::ABYTES + lw * 1024;
    constexpr int P = C::P;
    auto issueA = [&](int tt) {
        const unsigned t = stage_of<C::NT, C::NC>(tt, cls);
        const unsigned sb = ldsb + (unsigned)(tt % C::S) * C::STAGE;
#pragma unroll
        for (int p = 0; p < C::PA; ++p) {
            if constexpr (C::AROW) dma16<false>(sb + a_dst + p * 1024, (p & 1) ? var1 : var0, ra, a_src + p * 8 * (C::KFULL * 2) + t * 128);
            else dma16<true>(sb + a_dst + p * 1024, va, ra, a_src + (t * 4 * C::NSUB + p) * 1024);
        }
    };
    auto issueB = [&](int tt) {
        const unsigned t = stage_of<C::NT, C::NC>(tt, cls);
        const unsigned sb = ldsb + (unsigned)(tt % C::S) * C::STAGE;
#pragma unroll
        for (int u = 0; u < C::NSUB; ++u)
#pragma unroll
            for (int g = 0; g < C::NSEG; ++g) dma16<false>(sb + b_dst + (u * C::NSEG + g) * 4096, vb, rb, b_src + (t * C::NSUB + u) * 32 * C::PITCH + g * C::SEGSTEP);
    };
    auto use = [&](int tt) { side.use(tt); };
    auto issue = [&](int tt) { issueA(tt); issueB(tt); };
    static_assert(C::S == 3 || C::S == 4, "ring depth");
    if constexpr (HAS_G) {
        issueB(0); issueB(1);
        bar();
        side.pre();
        issueA(0); issueA(1);
        wait_vm<C::PA>();
    } else {
        issue(0);
        side.pre();
        issue(1);
        wait_vm<P + Side::NPRE>();
    }
    bar();
    issue(2);
    wait_vm<P>();
    side.landed();
    bar();
    use(0); use(1);
    int nx = 3;
    if (C::S == 4) { issue(3); nx = 4; }
    issue(nx); ++nx;
    for (; nx < C::NT; ++nx) {
        wait_vm<P*(C::S - 2)>();
        bar();
        use(nx - C::S + 1);
        issue(nx);
    }
    wait_vm<P*(C::S - 2)>(); bar(); use(C::NT - C::S + 1);
    if (C::S == 4) { wait_vm<P>(); bar(); use(C::NT - 2); }
    wait_vm<0>(); bar(); use(C::NT - 1);
}

template <class C> struct Frags { bf16x8 X[4]; bf16x4 W[C::NSEG][2][2]; };

template <class C, int TMASK>
struct Consumer {
    LAS char* lds; unsigned xa[2], wa[2][2];
    DI Consumer(LAS char* lds_, int cw, int lane) : lds(lds_) {
        const int wm = cw & 1, wn = cw >> 1;
        const int g = lane >> 4, fr = lane & 15, q = (lane & 15) >> 2, p = lane & 3;
        const unsigned rho = 16 * (g >> 1) + 4 * (g & 1) + q, xr = 2 * (g & 1) + (q >> 1);
#pragma unroll
        for (int pos = 0; pos < 2; ++pos) {
            const int u = C::AROW ? pos ^ wm : 0;
            xa[pos] = C::AROW ? (wm * 8192 + fr * 128 + ((g ^ (fr >> 1)) << 4)) ^ (u * 64) : wm * (4 * C::NSUB * 1024) + g * 1024 + fr * 16;
#pragma unroll
            for (int dt = 0; dt < 2; ++dt) wa[pos][dt] = C::ABYTES + u * (C::NSEG * 4096) + rho * 128 + (((2 * wn + dt) ^ xr) << 5) + (p >> 1) * 16 + (p & 1) * 8;
        }
    }
    static constexpr int NR = 4 + 4 * C::NSEG;
    static constexpr int NM = 8 * C::NSEG;
    DI void load1(Frags<C>& f, unsigned sb, int u, int i) const {
        if (i >= 2 && i < 6) {
            if constexpr (C::AROW) f.X[i - 2] = lds_b128(lds + sb + xa[u] + (i - 2) * 2048);
            else f.X[i - 2] = lds_b128(lds + sb + xa[0] + u * 4096 + (i - 2) * 256);
        } else {
            const int k = i < 2 ? i : i - 4, s = k >> 2, dt = (k >> 1) & 1, h = k & 1;
            if constexpr (C::AROW) f.W[s][dt][h] = lds_tr(lds + sb + wa[u][dt] + s * 4096 + h * 1024);
            else f.W[s][dt][h] = lds_tr(lds + sb + wa[0][dt] + (u * C::NSEG + s) * 4096 + h * 1024);
        }
    }
    DI static bf16x8 wfrag(const Frags<C>& f, int s, int dt) { return cat(f.W[s][dt][0], f.W[s][dt][1]); }
    DI void mma1(const Frags<C>& f, f32x4 (&acc)[C::NSEG][2][4], int i) const {
        const int s = i >> 3, dt = (i >> 2) & 1, mt = i & 3;
        if ((TMASK >> s) & 1) acc[s][dt][mt] = mfma(wfrag(f, s, dt), f.X[mt], acc[s][dt][mt]);
        else acc[s][dt][mt] = mfma(f.X[mt], wfrag(f, s, dt), acc[s][dt][mt]);
    }
};

template <class C, int TMASK, class Extra>
DI void split_consumer(Ctx& c, f32x4 (&acc)[C::NSEG][2][4], Extra extra) {
    typedef Consumer<C, TMASK> K;
    const K k(c.lds, c.w, c.lane);
    auto sub = [&](const Frags<C>& cur, Frags<C>& nxt, unsigned sb, int u, auto READ) {
#pragma unroll
        for (int i = 0; i < K::NM; ++i) {
            k.mma1(cur, acc, i);
            if constexpr (decltype(READ)::value) { if (i < K::NR) k.load1(nxt, sb, u, i); }
            SCHED();
        }
    };
    Frags<C> f0, f1;
    bar();
#pragma unroll
    for (int i = 0; i < K::NR; ++i) k.load1(f0, 0, 0, i);
    wait_lgkm0();
    unsigned slot = 0;
    static_assert(C::NSUB == 2, "NSUB");
    for (int t = 0; t < C::NT - 1; ++t) {
        const unsigned sb = slot * C::STAGE;
        slot = (slot + 1 == C::S) ? 0 : slot + 1;
        sub(f0, f1, sb, 1, T_{});
        extra(f0, t, std::integral_constant<int, 0>{});
        wait_lgkm0();
        bar();
        sub(f1, f0, slot * C::STAGE, 0, T_{});
        extra(f1, t, std::integral_constant<int, 1>{});
        wait_lgkm0();
    }
    sub(f0, f1, slot * C::STAGE, 1, T_{});
    extra(f0, C::NT - 1, std::integral_constant<int, 0>{});
    wait_lgkm0();
    sub(f1, f0, 0, 0, F_{});
    extra(f1, C::NT - 1, std::integral_constant<int, 1>{});
}

template <class C, int TMASK>
DI void split_consumer_stage(Ctx& c, f32x4 (&acc)[C::NSEG][2][4]) {
    typedef Consumer<C, TMASK> K;
    const K k(c.lds, c.w, c.lane);
    struct SF { Frags<C> u[C::NSUB]; };
    constexpr int NM = K::NM * C::NSUB, NR = K::NR * C::NSUB;
    auto step = [&](const SF& cur, SF& nxt, unsigned sb, auto READ, auto BARR) {
#pragma unroll
        for (int i = 0; i < NM; ++i) {
            k.mma1(cur.u[i / K::NM], acc, i % K::NM);
            if constexpr (decltype(READ)::value) {
                if (2 * i < NR) k.load1(nxt.u[(2 * i) / K::NR], sb, (2 * i) / K::NR, (2 * i) % K::NR);
                if (2 * i + 1 < NR) k.load1(nxt.u[(2 * i + 1) / K::NR], sb, (2 * i + 1) / K::NR, (2 * i + 1) % K::NR);
            }
            SCHED();
        }
        wait_lgkm0();
        if constexpr (decltype(BARR)::value) bar();
    };
    SF f0, f1;
    bar();
#pragma unroll
    for (int i = 0; i < NR; ++i) k.load1(f0.u[i / K::NR], 0, i / K::NR, i % K::NR);
    wait_lgkm0();
    bar();
    static_assert(C::NT % 2 == 0 && C::S == 4, "stage loop");
    for (int t = 0; t < C::NT - 2; t += 2) {
        step(f0, f1, ((t + 1) & 3) * C::STAGE, T_{}, T_{});
        step(f1, f0, ((t + 2) & 3) * C::STAGE, T_{}, T_{});
    }
    step(f0, f1, ((C::NT - 1) & 3) * C::STAGE, T_{}, F_{});
    step(f1, f0, 0, F_{}, F_{});
}

DI void warm_up(const Ctx& c, const bf16_t* wo, const bf16_t* wg, const bf16_t* wu, const bf16_t* wd) {
    const unsigned dummy = (unsigned)(size_t)c.lds + LDS_DUMMY;
    const int lane = lane_id();
    if (c.w == 1) touch(dummy, lane * 128, mk_rsrc(wo, DM * DM * 2), c.b * 8192);
    if (c.w == 2) touch(dummy, lane < 10 ? (lane * 256 + c.b) * 128 : 0x7fffff00u, mk_rsrc(c.ws + WS_FLAG, FLAG_BYTES), 0);
    if (c.w == 3) {
        const int idx = (c.b >> 3) * 12 + lane;
        const unsigned o = (unsigned)idx * 65536u;
        const bool on = lane < 12;
        touch(dummy, on && idx < 32 ? o : 0x7fffff00u, mk_rsrc(wo, DM * DM * 2), 0);
        touch(dummy, on && idx >= 32 && idx < 144 ? o - 32 * 65536u : 0x7fffff00u, mk_rsrc(wg, DM * FF * 2), 0);
        touch(dummy, on && idx >= 144 && idx < 256 ? o - 144 * 65536u : 0x7fffff00u, mk_rsrc(wu, DM * FF * 2), 0);
        touch(dummy, on && idx >= 256 && idx < 368 ? o - 256 * 65536u : 0x7fffff00u, mk_rsrc(wd, DM * FF * 2), 0);
    }
}

typedef SplitCfg<3, 2, 3, DM, DM, 3 * DM * 2, 2048, 4> CfgQKV;
typedef SplitCfg<3, 2, 3, DM, DM, 3 * DM * 2, 2048, 4, true> CfgQKV0;

DI int wo_fidx(int b) { const int xcd = b & 7, j = b >> 3, pm = 4 * (xcd & 3) + (j & 3), pn = 8 * (xcd >> 2) + (j >> 2); return (pm >> 1) * 32 + (pm & 1) * 16 + pn; }
struct StatsSide {
    static constexpr int NPRE = 5;
    LAS char* lds; char* ws; int w, lane, pm, hp; u32x4 sv[4]; unsigned cv;
    DI void pre() {
        const __amdgpu_buffer_rsrc_t rs = mk_rsrc(ws + WS_ST2, SEQ * 128), rc = mk_rsrc(ws + WS_CSQ, 3 * DM * 4);
#pragma unroll
        for (int i = 0; i < 4; ++i) asm volatile("buffer_load_dwordx4 %0, %1, %2, 0 offen sc1" : "=v"(sv[i]) : "v"(stats_off(pm * 128 + w * 32, lane, i)), "s"(rs) : "memory");
        const int cc = 48 * w + (lane < 48 ? lane : 0);
        asm volatile("buffer_load_dword %0, %1, %2, 0 offen sc1" : "=v"(cv) : "v"(((cc >> 6) * DM + hp * 64 + (cc & 63)) * 4), "s"(rc) : "memory");
    }
    DI void landed() { asm volatile("" : "+v"(sv[0]), "+v"(sv[1]), "+v"(sv[2]), "+v"(sv[3]), "+v"(cv)); }
    DI void use(int tt) {
        if (tt == 3) {
            landed();
            LAS f32x2* st = (LAS f32x2*)(lds + CfgQKV::RING);
            LAS float* cs = (LAS float*)(lds + CfgQKV::RING + 1024);
            const f32x4 r = row_stats(sv);
            if (lane < 16) *(LAS f32x4*)(st + w * 32 + 2 * lane) = r;
            if (lane < 48) cs[48 * w + lane] = __uint_as_float(cv);
            wait_lgkm0();
        }
    }
};
constexpr int QKV_LDS_ROWST = CfgQKV::RING + 4096;
struct StatsXSide {
    static constexpr int NPRE = 4;
    LAS char* lds; char* ws; const bf16_t* x; int w, lane, b, pm; u64 tag; u32x4 xa[2], xb[2];
    DI void pre() {
        const __amdgpu_buffer_rsrc_t rx = mk_rsrc(x, SEQ * DM * 2);
#pragma unroll
        for (int i = 0; i < 2; ++i) {
            asm volatile("buffer_load_dwordx4 %0, %1, %2, 0 offen" : "=v"(xa[i]) : "v"((b * 8 + w * 2 + i) * (DM * 2) + lane * 16), "s"(rx) : "memory");
            asm volatile("buffer_load_dwordx4 %0, %1, %2, 0 offen offset:1024" : "=v"(xb[i]) : "v"((b * 8 + w * 2 + i) * (DM * 2) + lane * 16), "s"(rx) : "memory");
        }
    }
    DI void landed() { asm volatile("" : "+v"(xa[0]), "+v"(xa[1]), "+v"(xb[0]), "+v"(xb[1])); }
    DI void load_flags() const {
        dma16<true>((unsigned)(size_t)lds + CfgQKV::RING + 3072, lane < 16 ? lane * 128 : 0x7fffff00u, mk_rsrc(ws + WS_FLAG, FLAG_BYTES), (F_XN * 256 + pm * 16) * 128);
    }
    DI bool flags_ok() const { return __all(*(LAS const u64*)(lds + CfgQKV::RING + 3072 + (lane & 15) * 16) == tag); }
    DI void use(int tt) {
        if (tt == 2) {
            landed();
#pragma unroll
            for (int i = 0; i < 2; ++i) {
                float v[16];
#pragma unroll
                for (int k = 0; k < 4; ++k) { v[2 * k] = bf_lo(xa[i][k]); v[2 * k + 1] = bf_hi(xa[i][k]); v[8 + 2 * k] = bf_lo(xb[i][k]); v[8 + 2 * k + 1] = bf_hi(xb[i][k]); }
                float s = 0.f;
#pragma unroll
                for (int k = 0; k < 16; ++k) s += v[k];
                float t = sum8(s); t += dpp<0x140>(t); t = xrow_sum(t);
                const float mean = t * (1.f / 1024.f);
                float q = 0.f;
#pragma unroll
                for (int k = 0; k < 16; ++k) { v[k] -= mean; q += v[k] * v[k]; }
                q = sum8(q); q += dpp<0x140>(q); q = xrow_sum(q);
                if (lane == 0) ((LAS f32x2*)(lds + QKV_LDS_ROWST))[w * 2 + i] = (f32x2){mean, __builtin_amdgcn_rsqf(q * (1.f / 1024.f) + LN_EPS)};
            }
            wait_lgkm0();
        }
        if (w != 0) return;
        if (tt == 11) load_flags();
        if (tt == 13) {
            if (!flags_ok()) {
                const u64 t0 = __builtin_amdgcn_s_memrealtime();
                do { load_flags(); wait_vm<0>(); } while (!flags_ok() && __builtin_amdgcn_s_memrealtime() - t0 < 2000000ull);
            }
            dma16<true>((unsigned)(size_t)lds + CfgQKV::RING, (lane >> 2) * 128 + (lane & 3) * 16, mk_rsrc(ws + WS_STX, 256 * 128), pm * 16 * 128);
        }
    }
};
template <int L>
DI void phase_qkv(Ctx& c, const bf16_t* __restrict__ x, const bf16_t* __restrict__ wqkv, const bf16_t* __restrict__ wo, const bf16_t* __restrict__ wg,
                  const bf16_t* __restrict__ wu, const bf16_t* __restrict__ wd) {
    typedef typename std::conditional<L == 0, CfgQKV0, CfgQKV>::type C;
    LAS char* lds = c.lds; char* ws = c.ws;
    const int lane = c.lane, w = c.w;
    const int xcd = c.b & 7, j = c.b >> 3;
    const int pm = 8 * (xcd & 1) + (j & 7), hp = 4 * (xcd >> 1) + (j >> 3);
    const int cls = (j + (j >> 3)) & 3;
    if (w >= 4) {
        if constexpr (L == 0) {
            StatsXSide side;
            side.lds = lds; side.ws = ws; side.x = x; side.w = w - 4; side.lane = lane; side.b = c.b; side.pm = pm; side.tag = c.tag;
            split_loader<C, false>(c, w - 4, cls, (const char*)x, SEQ * DM * 2, pm * 128 * (DM * 2), (const char*)wqkv, DM * 3 * DM * 2, hp * 128, side);
        } else {
            StatsSide side;
            side.lds = lds; side.ws = ws; side.w = w - 4; side.lane = lane; side.pm = pm; side.hp = hp;
            split_loader<C>(c, w - 4, cls, ws + WS_X2, SEQ * DM * 2, pm * 2 * (C::KC * 1024), (const char*)wqkv, DM * 3 * DM * 2, hp * 128, side);
        }
        bar();
        const int cw = w - 4, wm = cw & 1, hh = cw >> 1, g = lane >> 4, fr = lane & 15, head = hp * 2 + hh;
        const LAS f32x2* st = (const LAS f32x2*)(lds + C::RING);
        const LAS float* cs = (const LAS float*)(lds + C::RING + 1024);
        const LAS f32x4* qa = (const LAS f32x4*)(lds + C::STAGE) + cw * 512 + lane;
        const __amdgpu_buffer_rsrc_t rq = mk_rsrc(ws + WS_PQ, SEQ * DM * 2);
#pragma unroll
        for (int mt = 0; mt < 4; ++mt) {
            const f32x2 s = st[wm * 64 + 16 * mt + fr];
            const float nmean = -s.x, rstd = s.y * LOG2E;
            unsigned pk[2][2];
#pragma unroll
            for (int dt = 0; dt < 2; ++dt) {
                const f32x4 a = qa[(dt * 4 + mt) * 64];
#pragma unroll
                for (int i = 0; i < 2; ++i) {
                    f32x2 t = {a[2 * i], a[2 * i + 1]};
                    f32x2 cq = *(LAS const f32x2*)(cs + hh * 32 + 16 * dt + 4 * g + 2 * i);
                    if (L == 0) cq += *(LAS const f32x2*)(cs + 192 + hh * 32 + 16 * dt + 4 * g + 2 * i);
                    t = t + cq * nmean;
                    pk[dt][i] = cvt_pk2(phi2l(t * rstd));
                }
            }
            swap16(pk[0][0], pk[1][0]); swap16(pk[0][1], pk[1][1]);
            const int ch = ((g & 1) << 1) | (g >> 1);
            st16(rq, blk_off(pm * 128 + wm * 64 + 16 * mt + fr, head * 4 + ch, 128), (u32x4){pk[0][0], pk[0][1], pk[1][0], pk[1][1]});
        }
        bar();
        signal(c, F_QKV + F_PER_LAYER * L, hp * 16 + pm);
        return;
    }
    if constexpr (L == 1) {
        if (w == 0) wait_flags(c, F_DN, pm * 16, 16);
        bar();
    }
    const int wm = w & 1, hh = w >> 1, g = lane >> 4, fr = lane & 15;
    f32x4 acc[3][2][4];
#pragma unroll
    for (int s = 0; s < 3; ++s)
#pragma unroll
        for (int d = 0; d < 2; ++d)
#pragma unroll
            for (int m = 0; m < 4; ++m) acc[s][d][m] = (f32x4){0.f, 0.f, 0.f, 0.f};
    bf16x8 ones = {0x3F80, 0x3F80, 0x3F80, 0x3F80, 0x3F80, 0x3F80, 0x3F80, 0x3F80};
    asm volatile("" : "+v"(ones));
    f32x4 csum[6];
#pragma unroll
    for (int k = 0; k < 6; ++k) csum[k] = (f32x4){0.f, 0.f, 0.f, 0.f};
    LAS f32x2* st = (LAS f32x2*)(lds + C::RING);
    LAS float* cs = (LAS float*)(lds + C::RING + 1024);
    int ncall = 0;
    auto extra = [&](const Frags<C>& f, int t, auto POS) {
        if constexpr (L == 0) {
            typedef Consumer<C, 1> K;
            constexpr int k0 = 3 * decltype(POS)::value;
#pragma unroll
            for (int k = k0; k < k0 + 3; ++k) csum[k] = mfma(K::wfrag(f, k >> 1, k & 1), ones, csum[k]);
            const int n = ncall++;
            if (n == 0) warm_up(c, wo, wg, wu, wd);
            if (w == 0) {
                if (n == 6 && lane < 16) st8(mk_rsrc(ws + WS_STX, 256 * 128), c.b * 128 + lane * 8, ((LAS const u32x2*)(lds + QKV_LDS_ROWST))[lane & 7]);
                if (n == 12) { wait_vm<0>(); if (lane == 0) __hip_atomic_store((GAS u64*)(ws + WS_FLAG) + (F_XN * 256 + c.b) * FLAG_STRIDE, c.tag, __ATOMIC_RELAXED, __HIP_MEMORY_SCOPE_AGENT); }
            }
        }
        (void)t;
    };
    split_consumer<C, 1>(c, acc, extra);

    {
        LAS f32x4* qa = (LAS f32x4*)(lds + C::STAGE) + w * 512 + lane;
#pragma unroll
        for (int dt = 0; dt < 2; ++dt)
#pragma unroll
            for (int mt = 0; mt < 4; ++mt) qa[(dt * 4 + mt) * 64] = acc[0][dt][mt];
    }
    if (L == 0 && fr == 0) {
#pragma unroll
        for (int k = 0; k < 6; ++k) *(LAS f32x4*)(cs + 192 * wm + (k >> 1) * 64 + hh * 32 + 16 * (k & 1) + 4 * g) = csum[k];
    }
    wait_lgkm0();
    bar();
    if (L == 0 && pm == 0 && c.tid < 192) st4(mk_rsrc(ws + WS_CSQ, 3 * DM * 4), ((c.tid >> 6) * DM + hp * 64 + (c.tid & 63)) * 4, __float_as_uint(cs[c.tid] + cs[192 + c.tid]));
    const int head = hp * 2 + hh;
    bf16x8 kf[2][2], vf[2][2];
    {
        f32x2 nm[4][2], rs[4][2], rl[4][2];
#pragma unroll
        for (int mt = 0; mt < 4; ++mt)
#pragma unroll
            for (int i = 0; i < 2; ++i) {
                const f32x4 s = *(LAS const f32x4*)(st + wm * 64 + 16 * mt + 4 * g + 2 * i);
                nm[mt][i] = (f32x2){-s[0], -s[2]}; rs[mt][i] = (f32x2){s[1], s[3]}; rl[mt][i] = rs[mt][i] * LOG2E;
            }
#pragma unroll
        for (int dt = 0; dt < 2; ++dt) {
            float ck = cs[64 + hh * 32 + 16 * dt + fr], cvv = cs[128 + hh * 32 + 16 * dt + fr];
            if (L == 0) { ck += cs[192 + 64 + hh * 32 + 16 * dt + fr]; cvv += cs[192 + 128 + hh * 32 + 16 * dt + fr]; }
#pragma unroll
            for (int sp = 0; sp < 2; ++sp) {
                u32x4 ku, vu;
#pragma unroll
                for (int h2 = 0; h2 < 2; ++h2) {
                    const int mt = 2 * sp + h2;
#pragma unroll
                    for (int i = 0; i < 2; ++i) {
                        f32x2 tk = {acc[1][dt][mt][2 * i], acc[1][dt][mt][2 * i + 1]}, tv = {acc[2][dt][mt][2 * i], acc[2][dt][mt][2 * i + 1]};
                        tk = (tk + nm[mt][i] * ck) * rl[mt][i]; tv = (tv + nm[mt][i] * cvv) * rs[mt][i];
                        ku[2 * h2 + i] = cvt_pk2(phi2l(tk)); vu[2 * h2 + i] = cvt_pk2(tv);
                    }
                }
                kf[dt][sp] = __builtin_bit_cast(bf16x8, ku); vf[dt][sp] = __builtin_bit_cast(bf16x8, vu);
            }
        }
    }
    f32x4 kv[2][2], z[2];
#pragma unroll
    for (int dt = 0; dt < 2; ++dt) {
        z[dt] = mfma(kf[dt][0], ones, (f32x4){0.f, 0.f, 0.f, 0.f});
        z[dt] = mfma(kf[dt][1], ones, z[dt]);
#pragma unroll
        for (int et = 0; et < 2; ++et) {
            kv[dt][et] = mfma(kf[dt][0], vf[et][0], (f32x4){0.f, 0.f, 0.f, 0.f});
            kv[dt][et] = mfma(kf[dt][1], vf[et][1], kv[dt][et]);
        }
    }
    LAS f32x4* red = (LAS f32x4*)(lds + 2 * C::STAGE + hh * 8192);
    if (wm == 1) {
#pragma unroll
        for (int dt = 0; dt < 2; ++dt) {
#pragma unroll
            for (int et = 0; et < 2; ++et) red[(dt * 2 + et) * 64 + lane] = kv[dt][et];
            red[(4 + dt) * 64 + lane] = z[dt];
        }
        wait_lgkm0();
    }
    bar();
    if (wm == 0) {
        const __amdgpu_buffer_rsrc_t rk = mk_rsrc(ws + WS_KVP, 16 * NH * KVP_BYTES);
        const unsigned dst = (pm * NH + head) * KVP_BYTES;
#pragma unroll
        for (int dt = 0; dt < 2; ++dt) {
#pragma unroll
            for (int et = 0; et < 2; ++et) {
                const f32x4 v = kv[dt][et] + red[(dt * 2 + et) * 64 + lane];
                st8(rk, dst + ((dt * 2 + et) * 64 + lane) * 8, (u32x2){cvt_pk(v[0], v[1]), cvt_pk(v[2], v[3])});
            }
            z[dt] += red[(4 + dt) * 64 + lane];
        }
        if (fr < 2) st16(rk, dst + 2048 + (16 * fr + 4 * g) * 4, __builtin_bit_cast(u32x4, fr ? z[1] : z[0]));
    }
    signal(c, F_QKV + F_PER_LAYER * L, hp * 16 + pm);
}

template <int L>
DI void phase_attn(Ctx& c) {
    LAS char* sm = c.lds;
    char* ws = c.ws;
    const int tid = c.tid, lane = c.lane, w = c.w, g = lane >> 4, fr = lane & 15;
    const int xcd = c.b & 7, j = c.b >> 3;
    const int head = 4 * xcd + (j & 3), pb = j >> 2;
    if (w == 0) wait_flags(c, F_QKV + F_PER_LAYER * L, (head >> 1) * 16, 16);
    bar();
    const __amdgpu_buffer_rsrc_t rk = mk_rsrc(ws + WS_KVP, 16 * NH * KVP_BYTES), rq = mk_rsrc(ws + WS_PQ, SEQ * DM * 2);
    const int grp = tid >> 7, e = tid & 127;
    f32x4 lo = {0.f, 0.f, 0.f, 0.f}, hi = lo, zs = lo;
    bf16x8 qf[2];
    {
        u32x4 p[4], pz[4];
#pragma unroll
        for (int i = 0; i < 4; ++i) p[i] = ld16(rk, ((grp * 4 + i) * NH + head) * KVP_BYTES + e * 16);
        if (e < 8) {
#pragma unroll
            for (int i = 0; i < 4; ++i) pz[i] = ld16(rk, ((grp * 4 + i) * NH + head) * KVP_BYTES + 2048 + e * 16);
        }
#pragma unroll
        for (int mt = 0; mt < 2; ++mt) qf[mt] = __builtin_bit_cast(bf16x8, ld16(rq, blk_off(pb * 256 + w * 32 + 16 * mt + fr, head * 4 + g, 128)));
#pragma unroll
        for (int i = 0; i < 4; ++i) {
            lo += (f32x4){bf_lo(p[i][0]), bf_hi(p[i][0]), bf_lo(p[i][1]), bf_hi(p[i][1])};
            hi += (f32x4){bf_lo(p[i][2]), bf_hi(p[i][2]), bf_lo(p[i][3]), bf_hi(p[i][3])};
        }
        if (e < 8) {
#pragma unroll
            for (int i = 0; i < 4; ++i) zs += __builtin_bit_cast(f32x4, pz[i]);
        }
    }
    *(LAS f32x4*)(sm + 4096 + (grp * 256 + 2 * e) * 16) = lo;
    *(LAS f32x4*)(sm + 4096 + (grp * 256 + 2 * e + 1) * 16) = hi;
    if (e < 8) *(LAS f32x4*)(sm + 20480 + (grp * 8 + e) * 16) = zs;
    wait_lgkm0();
    bar();
    if (w < 4) {
        f32x4 s = {0.f, 0.f, 0.f, 0.f};
#pragma unroll
        for (int i = 0; i < 4; ++i) s += *(LAS const f32x4*)(sm + 4096 + (i * 256 + tid) * 16);
        const int dt = w >> 1, et = w & 1;
        *(LAS u32x2*)(sm + (16 * et + fr) * 80 + (16 * dt + 4 * g) * 2) = (u32x2){cvt_pk(s[0], s[1]), cvt_pk(s[2], s[3])};
    } else if (tid < 256 + 8) {
        f32x4 s = {0.f, 0.f, 0.f, 0.f};
#pragma unroll
        for (int i = 0; i < 4; ++i) s += *(LAS const f32x4*)(sm + 20480 + (i * 8 + tid - 256) * 16);
        *(LAS u32x2*)(sm + 2560 + (tid - 256) * 8) = (u32x2){cvt_pk(s[0], s[1]), cvt_pk(s[2], s[3])};
    }
    wait_lgkm0();
    bar();
    bf16x8 kf[2];
#pragma unroll
    for (int et = 0; et < 2; ++et) kf[et] = lds_b128(sm + (16 * et + fr) * 80 + g * 16);
    const bf16x8 zf = lds_b128(sm + 2560 + g * 16);
    const f32x4 zero = {0.f, 0.f, 0.f, 0.f};
    const __amdgpu_buffer_rsrc_t ro = mk_rsrc(ws + WS_O, SEQ * DM * 2);
#pragma unroll
    for (int mt = 0; mt < 2; ++mt) {
        const f32x4 den = mfma(zf, qf[mt], zero);
        const f32x4 n0 = mfma(kf[0], qf[mt], zero), n1 = mfma(kf[1], qf[mt], zero);
        const float r = __builtin_amdgcn_rcpf(den[0] + 1e-6f);
        unsigned a0 = cvt_pk(n0[0] * r, n0[1] * r), a1 = cvt_pk(n0[2] * r, n0[3] * r);
        unsigned b0 = cvt_pk(n1[0] * r, n1[1] * r), b1 = cvt_pk(n1[2] * r, n1[3] * r);
        swap16(a0, b0); swap16(a1, b1);
        const int ch = ((g & 1) << 1) | (g >> 1);
        st16(ro, blk_off(pb * 256 + w * 32 + 16 * mt + fr, head * 4 + ch, 128), (u32x4){a0, a1, b0, b1});
    }
    signal(c, F_ATT + F_PER_LAYER * L, pb * 32 + head);
}

template <bool OUT_RM>
DI void epi_n1024(Ctx& c, f32x4 (&acc)[2][4], const u32x4 (&rv)[4], int pm, int pn, char* out, char* stats) {
    const int lane = c.lane, w = c.w, wm = w & 1, wn = w >> 1, g = lane >> 4, fr = lane & 15;
    const int kc = pn * 8 + wn * 4 + (((g & 1) << 1) | (g >> 1));
    const __amdgpu_buffer_rsrc_t ro = mk_rsrc(out, SEQ * DM * 2);
    f32x2 part[4];
#pragma unroll
    for (int mt = 0; mt < 4; ++mt) {
        float v[8];
#pragma unroll
        for (int i = 0; i < 4; ++i) { float a = acc[0][mt][i], b = acc[1][mt][i]; swap16f(a, b); v[i] = a; v[4 + i] = b; }
        u32x4 o;
#pragma unroll
        for (int i = 0; i < 4; ++i) o[i] = cvt_pk(v[2 * i] + bf_lo(rv[mt][i]), v[2 * i + 1] + bf_hi(rv[mt][i]));
        const int row = pm * 128 + wm * 64 + 16 * mt + fr;
        (void)row;
        if (OUT_RM) *(LAS u32x4*)(c.lds + (wm * 64 + 16 * mt + fr) * 144 + (kc & 7) * 16) = o;
        else {
            st16(ro, blk_off(row, kc, 128), o);
            float s = 0.f;
#pragma unroll
            for (int i = 0; i < 4; ++i) { v[2 * i] = bf_lo(o[i]); v[2 * i + 1] = bf_hi(o[i]); s += v[2 * i] + v[2 * i + 1]; }
            const float m = xrow_sum(s) * (1.f / 32.f);
            float q = 0.f;
#pragma unroll
            for (int i = 0; i < 8; ++i) q += (v[i] - m) * (v[i] - m);
            part[mt] = (f32x2){m, xrow_sum(q)};
        }
    }
    if (OUT_RM) {
        wait_lgkm0();
        bar();
#pragma unroll
        for (int i = 0; i < 4; ++i) {
            const int r = (c.tid >> 3) + 32 * i, ch = c.tid & 7;
            st16(ro, (pm * 128 + r) * (DM * 2) + pn * 128 + ch * 16, *(LAS const u32x4*)(c.lds + r * 144 + ch * 16));
        }
    } else {
        LAS f32x2* sp = (LAS f32x2*)c.lds;
        if (wn == 1 && g == 0) {
#pragma unroll
            for (int mt = 0; mt < 4; ++mt) sp[wm * 64 + 16 * mt + fr] = part[mt];
        }
        wait_lgkm0();
        bar();
        if (wn == 0 && g == 0) {
#pragma unroll
            for (int mt = 0; mt < 4; ++mt) {
                const f32x2 o = sp[wm * 64 + 16 * mt + fr];
                const float d = part[mt].x - o.x;
                const f32x2 r = {0.5f * (part[mt].x + o.x), part[mt].y + o.y + 16.f * d * d};
                st8(mk_rsrc(stats, SEQ * 128), (pn * SEQ + pm * 128 + wm * 64 + 16 * mt + fr) * 8, __builtin_bit_cast(u32x2, r));
            }
        }
    }
}

DI int n1024_class(int j) { return (j + (j >> 2)) & 3; }
typedef SplitCfg<1, 2, 4, DM, DM, DM * 2, 0, 4> CfgWO;
template <int L>
DI void phase_wo(Ctx& c, const bf16_t* __restrict__ wo, const bf16_t* __restrict__ x) {
    typedef CfgWO C;
    char* ws = c.ws;
    const int lane = c.lane, w = c.w;
    const int xcd = c.b & 7, j = c.b >> 3;
    const int pm = 4 * (xcd & 3) + (j & 3), pn = 8 * (xcd >> 2) + (j >> 2);
    const int fidx = wo_fidx(c.b);
    if (w >= 4) {
        NoSide side;
        split_loader<C>(c, w - 4, n1024_class(j), ws + WS_O, SEQ * DM * 2, pm * 2 * (C::KC * 1024), (const char*)wo, DM * DM * 2, pn * 128, side);
        bar();
        signal(c, F_WO + F_PER_LAYER * L, fidx);
        return;
    }
    if (w == 0) wait_flags(c, F_ATT + F_PER_LAYER * L, (pm >> 1) * 32, 32);
    bar();
    const int wm = w & 1, wn = w >> 1, g = lane >> 4, fr = lane & 15;
    const int kc = pn * 8 + wn * 4 + (((g & 1) << 1) | (g >> 1));
    u32x4 rv[4];
#pragma unroll
    for (int mt = 0; mt < 4; ++mt) {
        const int row = pm * 128 + wm * 64 + 16 * mt + fr;
        rv[mt] = L == 0 ? *(const u32x4*)((const char*)x + (size_t)row * (DM * 2) + kc * 16) : ld16(mk_rsrc(ws + WS_X2, SEQ * DM * 2), blk_off(row, kc, 128));
    }
    f32x4 acc[1][2][4];
#pragma unroll
    for (int d = 0; d < 2; ++d)
#pragma unroll
        for (int m = 0; m < 4; ++m) acc[0][d][m] = (f32x4){0.f, 0.f, 0.f, 0.f};
    split_consumer_stage<C, 1>(c, acc);
    epi_n1024<false>(c, acc[0], rv, pm, pn, ws + WS_X1, ws + WS_ST1);
    signal(c, F_WO + F_PER_LAYER * L, fidx);
}

typedef SplitCfg<1, 2, 4, FF, FF, DM * 2, 0, 4> CfgDN;
template <int L>
DI void phase_down(Ctx& c, const bf16_t* __restrict__ wd, char* __restrict__ out) {
    typedef CfgDN C;
    constexpr bool OUT_RM = L == 1;
    char* ws = c.ws;
    const int lane = c.lane, w = c.w;
    const int xcd = c.b & 7, j = c.b >> 3;
    const int pm = 4 * (xcd & 3) + (j & 3), pn = 8 * (xcd >> 2) + (j >> 2);
    if (w >= 4) {
        NoSide side;
        split_loader<C>(c, w - 4, n1024_class(j), ws + WS_H, SEQ * FF * 2, pm * 2 * (C::KC * 1024), (const char*)wd, FF * DM * 2, pn * 128, side);
        bar();
        if (!OUT_RM) signal(c, F_DN, pm * 16 + pn);
        return;
    }
    if (w == 0) wait_flags(c, F_GU + F_PER_LAYER * L, (pm >> 1) * 32, 32);
    bar();
    const int wm = w & 1, wn = w >> 1, g = lane >> 4, fr = lane & 15;
    const int kc = pn * 8 + wn * 4 + (((g & 1) << 1) | (g >> 1));
    u32x4 rv[4];
#pragma unroll
    for (int mt = 0; mt < 4; ++mt) rv[mt] = ld16(mk_rsrc(ws + WS_X1, SEQ * DM * 2), blk_off(pm * 128 + wm * 64 + 16 * mt + fr, kc, 128));
    f32x4 acc[1][2][4];
#pragma unroll
    for (int d = 0; d < 2; ++d)
#pragma unroll
        for (int m = 0; m < 4; ++m) acc[0][d][m] = (f32x4){0.f, 0.f, 0.f, 0.f};
    split_consumer_stage<C, 1>(c, acc);
    epi_n1024<OUT_RM>(c, acc[0], rv, pm, pn, OUT_RM ? out : ws + WS_X2, ws + WS_ST2);
    if (!OUT_RM) signal(c, F_DN, pm * 16 + pn);
}

constexpr int GU_LEAD = 3, GU_NC = 4;
constexpr int GU_S = 4;
constexpr int GU_GOFF = 16384, GU_UOFF = 16384 + 7168 + 16, GU_STAGE = GU_UOFF + 7168;
constexpr int GU_RING = GU_S * GU_STAGE;
constexpr int GU_STATS = GU_RING, GU_CS = GU_STATS + 2048;
static_assert(GU_CS + 1024 <= LDS_DUMMY && CfgDN::RING <= LDS_DUMMY && CfgQKV::RING + 8192 <= LDS_DUMMY, "LDS map");
struct GuFrags { bf16x8 X[4]; bf16x4 W[7][2]; };

template <int L>
DI void gu_epilogue(Ctx& c, const f32x4 (&acc)[7][4], const f32x4& csa, const f32x4& csb, int pm, int pn) {
    constexpr bool FIRST = L == 0;
    LAS char* lds = c.lds; char* ws = c.ws;
    const int lane = lane_id(), w = c.w, tid = w * 64 + lane;
    const int wr = w >> 1, wc = w & 1, g = lane >> 4, fr = lane & 15;
    LAS f32x2* st = (LAS f32x2*)(lds + GU_STATS);
    LAS float* cs = (LAS float*)(lds + GU_CS);
    if constexpr (FIRST) {
        const int ca = (wr < 3 ? 16 * wr : 112) + 56 * wc + 4 * g;
        const int cb = wr < 2 ? 112 + 16 * (wr + 1) + 56 * wc + 4 * g : (g >> 1) * 112 + 56 * wc + 48 + 4 * (g & 1);
        if (fr == 0) {
#pragma unroll
            for (int i = 0; i < 4; ++i) { cs[ca + i] = csa[i]; if (wr < 3) cs[cb + i] = csb[i]; }
        }
        wait_lgkm0();
        bar();
        if (pm == 0 && tid < 256) st4(mk_rsrc(ws + WS_CSG, 32 * 1024), pn * 1024 + tid * 4, tid < 224 ? __float_as_uint(cs[tid]) : 0u);
    }
    auto swiglu2 = [&](f32x2 a, f32x2 b, float rl, float r2) {
        const f32x2 x = a * rl;
        const f32x2 d = (f32x2){__builtin_amdgcn_exp2f(x.x), __builtin_amdgcn_exp2f(x.y)} + 1.f;
        const f32x2 pr = (a * b) * r2;
        return pr * (f32x2){__builtin_amdgcn_rcpf(d.x), __builtin_amdgcn_rcpf(d.y)};
    };
    f32x2 cg[3][2], cu[3][2], cm[2];
#pragma unroll
    for (int T = 0; T < 3; ++T)
#pragma unroll
        for (int i = 0; i < 2; ++i) { cg[T][i] = *(LAS const f32x2*)(cs + 56 * wc + 16 * T + 4 * g + 2 * i); cu[T][i] = *(LAS const f32x2*)(cs + 112 + 56 * wc + 16 * T + 4 * g + 2 * i); }
#pragma unroll
    for (int i = 0; i < 2; ++i) cm[i] = *(LAS const f32x2*)(cs + (g >> 1) * 112 + 56 * wc + 48 + 4 * (g & 1) + 2 * i);
    const __amdgpu_buffer_rsrc_t rh = mk_rsrc(ws + WS_H, SEQ * FF * 2);
    const int kc0 = pn * 14 + wc * 7;
    unsigned P2[4][2], Mx[4];
#pragma unroll
    for (int m = 0; m < 4; ++m) {
        const f32x2 s = st[wr * 64 + 16 * m + fr];
        const float nm = -s.x, rl = -LOG2E * s.y, r2 = s.y * s.y;
        unsigned P[3][2];
#pragma unroll
        for (int T = 0; T < 3; ++T)
#pragma unroll
            for (int i = 0; i < 2; ++i) {
                const f32x2 a = (f32x2){acc[T][m][2 * i], acc[T][m][2 * i + 1]} + cg[T][i] * nm;
                const f32x2 b = (f32x2){acc[T + 3][m][2 * i], acc[T + 3][m][2 * i + 1]} + cu[T][i] * nm;
                const f32x2 h = swiglu2(a, b, rl, r2);
                P[T][i] = cvt_pk(h.x, h.y);
            }
        {
            const f32x2 v01 = (f32x2){acc[6][m][0], acc[6][m][1]} + cm[0] * nm, v23 = (f32x2){acc[6][m][2], acc[6][m][3]} + cm[1] * nm;
            float v0 = v01.x, v1 = v01.y, v2 = v23.x, v3 = v23.y;
            swap32f(v0, v2); swap32f(v1, v3);
            const f32x2 h = swiglu2((f32x2){v0, v1}, (f32x2){v2, v3}, rl, r2);
            Mx[m] = cvt_pk(h.x, h.y);
        }
        swap16(P[0][0], P[1][0]); swap16(P[0][1], P[1][1]);
        st16(rh, blk_off(pm * 256 + wr * 64 + 16 * m + fr, kc0 + 2 * (g & 1) + (g >> 1), FF / 8), (u32x4){P[0][0], P[0][1], P[1][0], P[1][1]});
        P2[m][0] = P[2][0]; P2[m][1] = P[2][1];
        if (m & 1) {
            swap16(P2[m - 1][0], P2[m][0]); swap16(P2[m - 1][1], P2[m][1]);
            st16(rh, blk_off(pm * 256 + wr * 64 + 16 * (m - 1 + (g & 1)) + fr, kc0 + 4 + (g >> 1), FF / 8), (u32x4){P2[m - 1][0], P2[m - 1][1], P2[m][0], P2[m][1]});
        }
    }
    {
        swap32(Mx[0], Mx[2]); swap32(Mx[1], Mx[3]);
        swap16(Mx[0], Mx[1]); swap16(Mx[2], Mx[3]);
        st16(rh, blk_off(pm * 256 + wr * 64 + 16 * g + fr, kc0 + 6, FF / 8), (u32x4){Mx[0], Mx[2], Mx[1], Mx[3]});
    }
    signal(c, F_GU + F_PER_LAYER * L, pm * 32 + pn);
}

template <int L>
DI void phase_gateup(Ctx& c, const bf16_t* __restrict__ wg, const bf16_t* __restrict__ wu, const bf16_t* __restrict__ wd) {
    constexpr bool FIRST = L == 0;
    LAS char* lds = c.lds; char* ws = c.ws;
    const int tid = c.tid, lane = c.lane, w = c.w;
    const int xcd = c.b & 7, jj = c.b >> 3;
    const int pm = 4 * (xcd & 1) + (jj & 3), pn = 8 * (xcd >> 1) + (jj >> 2);
    const int wr = w >> 1, wc = w & 1, g = lane >> 4, fr = lane & 15, q = fr >> 2, p = lane & 3;

    const __amdgpu_buffer_rsrc_t ra = mk_rsrc(ws + WS_X1, SEQ * DM * 2);
    const __amdgpu_buffer_rsrc_t rg = mk_rsrc(wg, w <= 6 ? DM * FF * 2 : 0), ru = mk_rsrc(wu, w >= 1 ? DM * FF * 2 : 0);
    const unsigned ldsb = (unsigned)(size_t)lds;
    const unsigned va = lane * 16;
    auto bpat = [&](int piece) { const unsigned i = 64 * piece + lane, rho = i / 14, cc = i % 14; const unsigned k = (rho & 0x13) | ((rho & 8) >> 1) | ((rho & 4) << 1); return k * (FF * 2) + cc * 16; };
    const unsigned vg = bpat(w <= 6 ? w : 0), vu = bpat(w >= 1 ? w - 1 : 0);
    const unsigned a_src = (pm * 4 + (w >> 1)) * (128 * 1024) + (w & 1) * 2048, a_dst = (w >> 1) * 4096 + (w & 1) * 2048;
    const unsigned g_dst0 = w <= 6 ? GU_GOFF + w * 1024 : LDS_DUMMY, g_str = w <= 6 ? GU_STAGE : 0;
    const unsigned u_dst0 = w >= 1 ? GU_UOFF + (w - 1) * 1024 : LDS_DUMMY, u_str = w >= 1 ? GU_STAGE : 0;
    const unsigned b_src = pn * 224;
    const int cls = ((jj & 3) + (jj >> 2)) & (GU_NC - 1);
    auto issue1 = [&](int ks, unsigned slot, int i) {
        if (i < 2) dma16<true>(ldsb + slot * GU_STAGE + a_dst + i * 1024, va, ra, a_src + (ks * 4 + i) * 1024);
        else if (i == 2) dma16<false>(ldsb + g_dst0 + slot * g_str, vg, rg, b_src + ks * (32 * FF * 2));
        else dma16<false>(ldsb + u_dst0 + slot * u_str, vu, ru, b_src + ks * (32 * FF * 2));
    };
    auto issue = [&](int t, int i0, int i1) {
        const int ks = stage_of<32, GU_NC>(t, cls);
#pragma unroll
        for (int i = 0; i < 4; ++i) if (i >= i0 && i < i1) issue1(ks, t & 3, i);
    };
    issue(0, 2, 4); issue(1, 2, 4);
    if (w == 0) wait_flags(c, F_WO + F_PER_LAYER * L, pm * 32, 32);
    bar();
    issue(0, 0, 2); issue(1, 0, 2);
    u32x4 sv[4]; float cv = 0.f;
    {
        const __amdgpu_buffer_rsrc_t rs = mk_rsrc(ws + WS_ST1, SEQ * 128), rc = mk_rsrc(ws + WS_CSG, 32 * 1024);
#pragma unroll
        for (int i = 0; i < 4; ++i) asm volatile("buffer_load_dwordx4 %0, %1, %2, 0 offen sc1" : "=v"(sv[i]) : "v"(stats_off(pm * 256 + w * 32, lane, i)), "s"(rs) : "memory");
        const unsigned co = pn * 1024 + (tid & 255) * 4;
        if (!FIRST) asm volatile("buffer_load_dword %0, %1, %2, 0 offen sc1" : "=v"(cv) : "v"(co), "s"(rc) : "memory");
    }
    constexpr int NX = FIRST ? 4 : 5;

    const unsigned xa = wr * 4096 + g * 1024 + fr * 16;
    const unsigned rho0 = 16 * (g >> 1) + 4 * (g & 1) + q;
    const unsigned wa = (rho0 * 14 + 7 * wc + (p >> 1)) * 16 + (p & 1) * 8;
    const unsigned wm_ = (rho0 * 14 + 7 * wc + 6) * 16 + (p & 1) * 8 + ((p >> 1) ? GU_UOFF : GU_GOFF);
    auto tile_addr = [&](int T) { return T < 3 ? wa + GU_GOFF + T * 32 : (T < 6 ? wa + GU_UOFF + (T - 3) * 32 : wm_); };
    const unsigned ca0 = tile_addr(wr), ca1 = tile_addr(wr < 3 ? wr + 4 : 3);
    auto load1 = [&](GuFrags& f, unsigned sb, int i) {
        if (i >= 2 && i < 6) f.X[i - 2] = lds_b128(lds + sb + xa + (i - 2) * 256);
        else {
            const int k = i < 2 ? i : i - 4, T = k >> 1, h = k & 1;
            LAS const char* pw = T < 3 ? lds + sb + wa + GU_GOFF + T * 32 : (T < 6 ? lds + sb + wa + GU_UOFF + (T - 3) * 32 : lds + sb + wm_);
            f.W[T][h] = lds_tr(pw + h * 1792);
        }
    };

    wait_vm<2 + NX>();
    bar();
    GuFrags f0, f1;
#pragma unroll
    for (int i = 0; i < 18; ++i) load1(f0, 0, i);
    issue(2, 0, 4);
    wait_vm<4 * (GU_LEAD - 2)>();
    asm volatile("" : "+v"(sv[0]), "+v"(sv[1]), "+v"(sv[2]), "+v"(sv[3]), "+v"(cv));
    wait_lgkm0();
    bar();

    f32x4 acc[7][4];
#pragma unroll
    for (int T = 0; T < 7; ++T)
#pragma unroll
        for (int m = 0; m < 4; ++m) acc[T][m] = (f32x4){0.f, 0.f, 0.f, 0.f};

    f32x4 csa = {0.f, 0.f, 0.f, 0.f}, csb = {0.f, 0.f, 0.f, 0.f};
    bf16x8 ones = {0x3F80, 0x3F80, 0x3F80, 0x3F80, 0x3F80, 0x3F80, 0x3F80, 0x3F80};
    if (FIRST) asm volatile("" : "+v"(ones));
    static_assert(GU_LEAD == 3, "the column-sum reads use the slot of the current stage (not the DMA destination); the loop bounds assume it too");
    auto step = [&](const GuFrags& cur, GuFrags& nxt, int t, auto DMA, auto READ, auto VM) {
        const unsigned nsb = ((t + 1) & (GU_S - 1)) * GU_STAGE;
        const unsigned dslot = (t + GU_LEAD) & (GU_S - 1);
        const int ks = stage_of<32, GU_NC>(t + GU_LEAD, cls);
        const unsigned csb_ = (t & (GU_S - 1)) * GU_STAGE;
        bf16x4 wcs[2][2];
#pragma unroll
        for (int i = 0; i < 28; ++i) {
            const int T = i >> 2, m = i & 3;
            acc[T][m] = mfma(cat(cur.W[T][0], cur.W[T][1]), cur.X[m], acc[T][m]);
            if constexpr (FIRST) {
                if (i >= 18 && i < 22) wcs[(i - 18) >> 1][i & 1] = lds_tr(lds + csb_ + (i < 20 ? ca0 : ca1) + (i & 1) * 1792);
                if (i == 26) csa = mfma(cat(wcs[0][0], wcs[0][1]), ones, csa);
                if (i == 27) csb = mfma(cat(wcs[1][0], wcs[1][1]), ones, csb);
            }
            if constexpr (decltype(READ)::value) { if (i < 18) load1(nxt, nsb, i); }
            if constexpr (decltype(DMA)::value) { if (i == 3 || i == 9 || i == 15 || i == 21) issue1(ks, dslot, (i - 3) / 6); }
            SCHED();
        }
        if constexpr (decltype(VM)::value >= 0) {
            wait_vm<(decltype(VM)::value >= 0 ? decltype(VM)::value : 0)>();
            wait_lgkm0();
            bar();
        } else wait_lgkm0();
    };
    LAS f32x2* st = (LAS f32x2*)(lds + GU_STATS);
    LAS float* cs = (LAS float*)(lds + GU_CS);
    for (int t = 0; t < 30; t += 2) {
        step(f0, f1, t, T_{}, T_{}, std::integral_constant<int, 4 * (GU_LEAD - 2)>{});
        if (t == 0) {
            asm volatile("" : "+v"(sv[0]), "+v"(sv[1]), "+v"(sv[2]), "+v"(sv[3]), "+v"(cv));
            const f32x4 r = row_stats(sv);
            if (lane < 16) *(LAS f32x4*)(st + w * 32 + 2 * lane) = r;
            if (!FIRST && tid < 224) cs[tid] = cv;
        }
        step(f1, f0, t + 1, T_{}, T_{}, std::integral_constant<int, 4 * (GU_LEAD - 2)>{});
    }
    if (FIRST && w >= 4) { const unsigned l = (w - 4) * 64 + lane; touch(ldsb + LDS_DUMMY, l < 224 ? l * 128 : 0x7fffff00u, mk_rsrc(wd, FF * DM * 2), c.b * 28672); }
    if (w < 4) __builtin_amdgcn_s_setprio(1);
    int tend = 32;
    asm volatile("" : "+s"(tend));
    for (int t = 30; t < tend; t += 2) {
        step(f0, f1, t, F_{}, T_{}, std::integral_constant<int, -1>{});
        step(f1, f0, t + 1, F_{}, F_{}, std::integral_constant<int, -1>{});
    }
    __builtin_amdgcn_s_setprio(0);
    gu_epilogue<L>(c, acc, csa, csb, pm, pn);
}

__global__ __launch_bounds__(512, 2) void k_mega(const bf16_t* __restrict__ x, const bf16_t* __restrict__ wqkv, const bf16_t* __restrict__ wo,
                                                 const bf16_t* __restrict__ wg, const bf16_t* __restrict__ wu, const bf16_t* __restrict__ wd,
                                                 char* __restrict__ out, char* __restrict__ ws) {
    extern __shared__ __attribute__((aligned(16))) char smem[];
    Ctx c;
    c.lds = (LAS char*)smem; c.ws = ws; c.tid = threadIdx.x; c.lane = threadIdx.x & 63; c.w = __builtin_amdgcn_readfirstlane(threadIdx.x >> 6); c.b = blockIdx.x;
    {
        u64 e;
        asm volatile("s_load_dwordx2 %0, %1, 0x0\n\ts_waitcnt lgkmcnt(0)" : "=s"(e) : "s"(ws + WS_EPOCH) : "memory");
        c.tag = e + 1;
    }
    fresh(c); phase_qkv<0>(c, x, wqkv, wo, wg, wu, wd);
    fresh(c); phase_attn<0>(c);
    fresh(c); phase_wo<0>(c, wo, x);
    fresh(c); phase_gateup<0>(c, wg, wu, wd);
    fresh(c); phase_down<0>(c, wd, out);
    fresh(c); phase_qkv<1>(c, x, wqkv, wo, wg, wu, wd);
    fresh(c); phase_attn<1>(c);
    fresh(c); phase_wo<1>(c, wo, x);
    fresh(c); phase_gateup<1>(c, wg, wu, wd);
    fresh(c); phase_down<1>(c, wd, out);
    if (c.b == 0 && c.tid == 0) __hip_atomic_store((GAS u64*)(ws + WS_EPOCH), c.tag, __ATOMIC_RELAXED, __HIP_MEMORY_SCOPE_AGENT);
}

}

extern "C" void kernel_launch(void* const* d_in, const int* in_sizes, int n_in, void* d_out, int out_size, void* d_ws, size_t ws_size, hipStream_t stream) {
    (void)in_sizes; (void)n_in; (void)out_size;
    const size_t ws_off = (ws_size - 44 * MB) & ~(size_t)(2 * MB - 1);
    (void)hipFuncSetAttribute((const void*)k_mega, hipFuncAttributeMaxDynamicSharedMemorySize, LDS_BYTES);
    k_mega<<<256, 512, LDS_BYTES, stream>>>((const bf16_t*)d_in[0], (const bf16_t*)d_in[1], (const bf16_t*)d_in[2], (const bf16_t*)d_in[4],
                                            (const bf16_t*)d_in[5], (const bf16_t*)d_in[6], (char*)d_out, (char*)d_ws + ws_off);
}
```
